```python
import torch

def _softmax(x, axis=-1):
    return torch.softmax(x, dim=axis)
def _gelu(x):
    return torch.nn.functional.gelu(x, approximate="tanh")
def _silu(x):
    return torch.nn.functional.silu(x)
def _sigmoid(x):
    return torch.sigmoid(x)
def _layernorm(x, axis=-1, eps=1e-5):
    m = x.mean(dim=axis, keepdim=True)
    v = x.var(dim=axis, keepdim=True, unbiased=False)
    return (x - m) / torch.sqrt(v + eps)
def _scatter_add(tgt, idx, src, axis=0):
    return tgt.clone().index_add_(axis, idx, src)
def _segment_sum(src, seg, n):
    out = torch.zeros((n,) + src.shape[1:], dtype=src.dtype)
    return out.index_add_(0, seg, src)
def _pad(x, pw):
    flat = tuple(v for pair in reversed(pw) for v in pair)
    return torch.nn.functional.pad(x, flat)

kernel_name = 'sequential_preLN__grouped_kv_mqa__glu_gated_geglu__prompt'

def setup_inputs():

    _w = (lambda a: (lambda t: t.to(torch.bfloat16) if t.is_floating_point() else t)(torch.as_tensor(a)))
    def _A_setup():
        B, S, D, H = 1, 2048, 512, 32
        Dh = D // H
        return {
            "x": _w((lambda *s: torch.randn(*s, dtype=torch.bfloat16) * 0.05)(B, S, D)),
            "wq": _w((lambda *s: torch.randn(*s, dtype=torch.bfloat16) * 0.05)(D, H * Dh)),
            "wk": _w((lambda *s: torch.randn(*s, dtype=torch.bfloat16) * 0.05)(D, Dh)),
            "wv": _w((lambda *s: torch.randn(*s, dtype=torch.bfloat16) * 0.05)(D, Dh)),
            "wo": _w((lambda *s: torch.randn(*s, dtype=torch.bfloat16) * 0.05)(H * Dh, D)),
        }
    _A_in = _A_setup()

    def _B_setup():
        B, S, Dm, Df = 1, 2048, 512, 1792
        return {
            "x": _w((lambda *s: torch.randn(*s, dtype=torch.bfloat16) * 0.05)(B, S, Dm)),
            "w_gate": _w((lambda *s: torch.randn(*s, dtype=torch.bfloat16) * 0.4)(Dm, Df) * (Dm ** -0.5)),
            "w_up": _w((lambda *s: torch.randn(*s, dtype=torch.bfloat16) * 0.4)(Dm, Df) * (Dm ** -0.5)),
            "w_down": _w((lambda *s: torch.randn(*s, dtype=torch.bfloat16) * 0.4)(Df, Dm) * (Df ** -0.5)),
        }
    _B_in = _B_setup()

    return {
        "A_x": _A_in["x"],
        "A_wq": _A_in["wq"],
        "A_wk": _A_in["wk"],
        "A_wv": _A_in["wv"],
        "A_wo": _A_in["wo"],
        "B_x": _B_in["x"],
        "B_w_gate": _B_in["w_gate"],
        "B_w_up": _B_in["w_up"],
        "B_w_down": _B_in["w_down"],
    }

def reference(A_x, A_wq, A_wk, A_wv, A_wo, B_x, B_w_gate, B_w_up, B_w_down):
    def _A_ref(x, wq, wk, wv, wo):
        B, S, D = x.shape
        H = 32
        Dh = D // H
        q = (lambda x, *d: x.permute(*d))((x @ wq).reshape(B, S, H, Dh), 0, 2, 1, 3)
        k = x @ wk
        v = x @ wv
        scores = torch.einsum("bhqd,bkd->bhqk", q, k) * (Dh ** -0.5)
        p = _softmax(scores, axis=-1)
        o = torch.einsum("bhqk,bkd->bhqd", p, v)
        return (lambda x, *d: x.permute(*d))(o, 0, 2, 1, 3).reshape(B, S, H * Dh) @ wo

    def _B_ref(x, w_gate, w_up, w_down):
        g = torch.einsum("bsd,df->bsf", x, w_gate)
        u = torch.einsum("bsd,df->bsf", x, w_up)
        h = _gelu(g) * u
        return torch.einsum("bsf,fd->bsd", h, w_down)

    x = A_x
    h = x + _A_ref(_layernorm(x, axis=-1), A_wq, A_wk, A_wv, A_wo)
    h2 = h + _B_ref(_layernorm(h, axis=-1), B_w_gate, B_w_up, B_w_down)
    return h2
```

```cpp
#include <hip/hip_runtime.h>
#include <hip/hip_bf16.h>
#include <stdint.h>

typedef unsigned short u16;
typedef __attribute__((ext_vector_type(8))) short bf16x8;
typedef __attribute__((ext_vector_type(4))) short s16x4;
typedef __attribute__((ext_vector_type(16))) float f32x16;
typedef __attribute__((ext_vector_type(4))) float f32x4;
typedef __attribute__((ext_vector_type(2))) float f32x2;
typedef __attribute__((ext_vector_type(4))) unsigned u32x4;
typedef __attribute__((ext_vector_type(2))) unsigned u32x2;
typedef __attribute__((ext_vector_type(2))) __bf16 bf16x2_t;

#define DI __device__ __forceinline__
#define LDSP __attribute__((address_space(3)))
typedef LDSP const char* lds_cptr;
typedef LDSP char* lds_ptr;

constexpr int S = 2048, D = 512, DF = 1792;
constexpr float LN_EPS = 1e-5f;
constexpr float QSCALE = 0.25f * 1.4426950408889634f;

DI unsigned pack_bf16(float a, float b) { f32x2 v = {a, b}; bf16x2_t r = __builtin_convertvector(v, bf16x2_t); return __builtin_bit_cast(unsigned, r); }
DI float bf_lo(unsigned u) { return __uint_as_float(u << 16); }
DI float bf_hi(unsigned u) { return __uint_as_float(u & 0xffff0000u); }
DI f32x16 mfma32(bf16x8 a, bf16x8 b, f32x16 c) { return __builtin_amdgcn_mfma_f32_32x32x16_bf16(a, b, c, 0, 0, 0); }
DI f32x4 mfma16(bf16x8 a, bf16x8 b, f32x4 c) { return __builtin_amdgcn_mfma_f32_16x16x32_bf16(a, b, c, 0, 0, 0); }
DI void glds16(const void* g, unsigned lds_base) {
    unsigned sv;
    asm volatile("s_mov_b32 %0, m0\n\ts_mov_b32 m0, %2\n\ts_nop 0\n\tglobal_load_lds_dwordx4 %1, off\n\ts_mov_b32 m0, %0"
                 : "=&s"(sv) : "v"(g), "s"(lds_base) : "memory");
}
DI void glds16x4(const void* g, unsigned lds_base) {
    unsigned sv;
    asm volatile("s_mov_b32 %0, m0\n\ts_mov_b32 m0, %2\n\ts_nop 0\n\tglobal_load_lds_dwordx4 %1, off\n\tglobal_load_lds_dwordx4 %1, off offset:1024\n\t"
                 "global_load_lds_dwordx4 %1, off offset:2048\n\tglobal_load_lds_dwordx4 %1, off offset:3072\n\ts_mov_b32 m0, %0"
                 : "=&s"(sv) : "v"(g), "s"(lds_base) : "memory");
}
DI unsigned rfl(unsigned v) { return (unsigned)__builtin_amdgcn_readfirstlane((int)v); }
DI s16x4 trread(lds_cptr p) { return __builtin_amdgcn_ds_read_tr16_b64_v4i16((LDSP s16x4*)p); }
DI bf16x8 trfrag(lds_cptr p0, lds_cptr p1) { s16x4 a = trread(p0), b = trread(p1); return __builtin_shufflevector(a, b, 0, 1, 2, 3, 4, 5, 6, 7); }
DI bf16x8 ldsfrag(lds_cptr p) { return *(LDSP const bf16x8*)p; }
#define SBAR() __builtin_amdgcn_sched_barrier(0)
#define PIN(x) asm volatile("" : "+v"(x))
#define EX(x) __builtin_amdgcn_exp2f(x)
#define GLD16(dst, ptr, imm) asm volatile("global_load_dwordx4 %0, %1, off offset:%2" : "=v"(dst) : "v"(ptr), "i"(imm))
#define GLD8(dst, ptr, imm) asm volatile("global_load_dwordx2 %0, %1, off offset:%2" : "=v"(dst) : "v"(ptr), "i"(imm))
#define ABL4 0
#define TS(k, i)
#define WAIT_VM(N) asm volatile("s_waitcnt vmcnt(" #N ")" ::: "memory")
#define WAIT_VM_BAR(N) asm volatile("s_waitcnt vmcnt(" #N ") lgkmcnt(0)\n\ts_barrier" ::: "memory")

DI void ln_rows8(const u32x4 (&v)[8], int lane, lds_ptr tile, int row0) {
    const int rr = lane >> 3, cc = lane & 7;
    f32x2 f[32];
#pragma unroll
    for (int c = 0; c < 8; ++c)
#pragma unroll
        for (int e = 0; e < 4; ++e) f[4 * c + e] = f32x2{bf_lo(v[c][e]), bf_hi(v[c][e])};
    f32x2 s2 = f[0];
#pragma unroll
    for (int i = 1; i < 32; ++i) s2 += f[i];
    float s = s2[0] + s2[1];
    s += __shfl_xor(s, 1); s += __shfl_xor(s, 2); s += __shfl_xor(s, 4);
    const float mean = s * (1.f / D);
    const f32x2 m2 = {mean, mean};
    f32x2 q2 = {0.f, 0.f};
#pragma unroll
    for (int i = 0; i < 32; ++i) { f[i] -= m2; q2 += f[i] * f[i]; }
    float q = q2[0] + q2[1];
    q += __shfl_xor(q, 1); q += __shfl_xor(q, 2); q += __shfl_xor(q, 4);
    const float rstd = 1.f / sqrtf(q * (1.f / D) + LN_EPS);
    const f32x2 r2 = {rstd, rstd};
    const int row = row0 + rr;
#pragma unroll
    for (int c = 0; c < 8; ++c) {
        u32x4 o;
#pragma unroll
        for (int e = 0; e < 4; ++e) { const f32x2 t = f[4 * c + e] * r2; o[e] = pack_bf16(t[0], t[1]); }
        *(LDSP u32x4*)(tile + row * 1024 + (((cc + 8 * c) ^ (row & 15)) << 4)) = o;
    }
}

constexpr int K1_XN = 0, K1_WQ = 65536, K1_WKV = 131072, K1_ST = 147456, K1_LDS = K1_ST + 9216;
__global__ __launch_bounds__(512, 2) void k1_ln_qkv(const u16* __restrict__ x, const u16* __restrict__ wq, const u16* __restrict__ wk,
                                                  const u16* __restrict__ wv, const u16* __restrict__ wo, const u16* __restrict__ wd,
                                                  u16* __restrict__ Qf, u16* __restrict__ Kf, u16* __restrict__ Vf, float* __restrict__ kmaxp,
                                                  u16* __restrict__ WoT, u16* __restrict__ WdT) {
    extern __shared__ __attribute__((aligned(16))) char smem[];
    const unsigned lds0 = (unsigned)(uintptr_t)smem;
    lds_ptr L = (lds_ptr)smem;
    const int tid = threadIdx.x, lane = tid & 63, w = (int)rfl(tid >> 6);
    const int bid = blockIdx.x, xcd = bid & 7, idx = bid >> 3, ib = xcd * 4 + (idx >> 3), j = idx & 7;
    const int row0 = ib * 64;
    TS(0, 0);
    u32x4 xv[8];
    {
        const u16* p = x + (size_t)(row0 + 8 * w + (lane >> 3)) * D + 8 * (lane & 7);
        GLD16(xv[0], p, 0); GLD16(xv[1], p, 128); GLD16(xv[2], p, 256); GLD16(xv[3], p, 384);
        GLD16(xv[4], p, 512); GLD16(xv[5], p, 640); GLD16(xv[6], p, 768); GLD16(xv[7], p, 896);
    }
    {
        const u16* src = wq + (size_t)((lane >> 2) & 7) * D + 64 * j + 32 * (lane >> 5) + 8 * (lane & 3);
#pragma unroll
        for (int n = 0; n < 8; ++n) { const int nn = w * 8 + n; glds16(src + (size_t)nn * 8 * D, lds0 + K1_WQ + nn * 1024); }
        const u16* s2 = ((j & 1) ? wv : wk) + lane * 8;
#pragma unroll
        for (int n = 0; n < 2; ++n) { const int nn = w * 2 + n; glds16(s2 + nn * 512, lds0 + K1_WKV + nn * 1024); }
        const int nt = bid & 15, sg = bid >> 4;
        {
            const u16* p = w < 7 ? wd + (size_t)(112 * sg + 16 * w + (lane >> 2)) * D + 32 * nt + 8 * (lane & 3)
                                 : wo + (size_t)(32 * sg + (lane >> 2)) * D + 32 * nt + 8 * (lane & 3);
            glds16(p, lds0 + K1_ST + w * 1024);
            if (w == 7) glds16(p + (size_t)16 * D, lds0 + K1_ST + 8 * 1024);
        }
    }
    TS(0, 1);
    WAIT_VM(11);
#pragma unroll
    for (int c = 0; c < 8; ++c) PIN(xv[c]);
    ln_rows8(xv, lane, L + K1_XN, 8 * w);
    TS(0, 2);
    WAIT_VM_BAR(0);
    TS(0, 3);
    const int r = lane & 31, h = lane >> 5, blk = (lane >> 4) & 1, q4 = (lane & 15) >> 2, p4 = lane & 3;
    const int wm = w & 1, wn = (w >> 1) & 1, kh = w >> 2;
    f32x16 acc = {};
    f32x4 akv = {};
    const int trow = 32 * wm + r;
    {
        lds_cptr wb = L + K1_WQ + kh * 32768 + h * 1024 + wn * 512 + q4 * 64 + 32 * blk + 8 * p4;
        lds_cptr xb = L + K1_XN + trow * 1024 + kh * 512;
        bf16x8 fa[16], fb[16];
#pragma unroll
        for (int i = 0; i < 16; ++i) { fa[i] = trfrag(wb + i * 2048, wb + i * 2048 + 256); fb[i] = ldsfrag(xb + ((((2 * i + h) & 15) ^ (trow & 15)) << 4) + (i >> 3) * 256); }
        const int g4 = lane >> 4, c16 = lane & 15, krow = 16 * (j >> 1) + c16;
        bf16x8 wf[2], xf[2];
#pragma unroll
        for (int ss = 0; ss < 2; ++ss) {
            const int s = 2 * w + ss;
            lds_cptr wp = L + K1_WKV + (32 * s + 8 * g4 + q4) * 32 + 8 * p4;
            wf[ss] = trfrag(wp, wp + 128);
            xf[ss] = ldsfrag(L + K1_XN + krow * 1024 + (((4 * s + g4) ^ (krow & 15)) << 4));
        }
#pragma unroll
        for (int i = 0; i < 16; ++i) acc = mfma32(fa[i], fb[i], acc);
#pragma unroll
        for (int ss = 0; ss < 2; ++ss) akv = (j & 1) ? mfma16(xf[ss], wf[ss], akv) : mfma16(wf[ss], xf[ss], akv);
    }
    TS(0, 4);
    {
        const int nt = bid & 15, sg = bid >> 4;
        if (w < 7) {
            lds_cptr p = L + K1_ST + w * 1024 + (4 * h + q4) * 64 + 32 * blk + 8 * p4;
            bf16x8 f = trfrag(p, p + 512);
            *(bf16x8*)(WdT + ((size_t)nt * 112 + 7 * sg + w) * 512 + lane * 8) = f;
        } else {
            const int g4 = lane >> 4;
#pragma unroll
            for (int n = 0; n < 2; ++n) {
                lds_cptr p = L + K1_ST + 7 * 1024 + (8 * g4 + q4) * 64 + 32 * n + 8 * p4;
                bf16x8 f = trfrag(p, p + 256);
                *(bf16x8*)(WoT + ((size_t)(2 * nt + n) * 16 + sg) * 512 + lane * 8) = f;
            }
        }
    }
    asm volatile("s_waitcnt lgkmcnt(0)\n\ts_barrier" ::: "memory");
    {
        LDSP f32x4* ex = (LDSP f32x4*)(L + K1_WQ + w * 2048 + lane * 16);
        const int o = kh ? 0 : 8;
        f32x4 e0, e1;
#pragma unroll
        for (int e = 0; e < 4; ++e) { e0[e] = kh ? acc[e] : acc[8 + e]; e1[e] = kh ? acc[4 + e] : acc[12 + e]; }
        (void)o;
        ex[0] = e0; ex[64] = e1;
        *(LDSP f32x4*)(L + K1_WQ + 16384 + w * 1024 + lane * 16) = akv;
    }
    __syncthreads();
    {
        LDSP const f32x4* ex = (LDSP const f32x4*)(L + K1_WQ + (w ^ 4) * 2048 + lane * 16);
        const f32x4 e0 = ex[0], e1 = ex[64];
        const int grow = row0 + trow;
#pragma unroll
        for (int gg = 0; gg < 2; ++gg) {
            const int g = 2 * kh + gg;
            float v[4];
#pragma unroll
            for (int e = 0; e < 4; ++e) v[e] = ((kh ? acc[8 + 4 * gg + e] : acc[4 * gg + e]) + (gg ? e1[e] : e0[e])) * QSCALE;
            const int head = (64 * j + 32 * wn) / 16 + (g >> 1);
            u32x2 o; o[0] = pack_bf16(v[0], v[1]); o[1] = pack_bf16(v[2], v[3]);
            u16* dst = Qf + ((((size_t)(grow >> 3) * 8 + (head >> 2)) * 2 + (g & 1)) * 32 + (head & 3) * 8 + (grow & 7)) * 8 + 4 * h;
            *(u32x2*)dst = o;
        }
    }
    if (w == 0) {
        const int g4 = lane >> 4, c16 = lane & 15;
        f32x4 t = akv;
#pragma unroll
        for (int ww = 1; ww < 8; ++ww) { f32x4 u = *(LDSP const f32x4*)(L + K1_WQ + 16384 + ww * 1024 + lane * 16); t += u; }
        u32x2 o; o[0] = pack_bf16(t[0], t[1]); o[1] = pack_bf16(t[2], t[3]);
        const int tok0 = row0 + 16 * (j >> 1);
        if (j & 1) {
            const int kb = tok0 >> 5, s = (tok0 >> 4) & 1;
            *(u32x2*)(Vf + ((((size_t)kb * 2 + s) * 2 + (g4 & 1)) * 16 + c16) * 8 + 4 * (g4 >> 1)) = o;
        } else {
            const int key = tok0 + c16;
            *(u32x2*)(Kf + (((size_t)(key >> 5) * 2 + (g4 >> 1)) * 32 + (key & 31)) * 8 + 4 * (g4 & 1)) = o;
            float a = bf_lo(o[0]), b = bf_hi(o[0]), c = bf_lo(o[1]), d = bf_hi(o[1]);
            float n2 = a * a + b * b + c * c + d * d;
            n2 += __shfl_xor(n2, 16); n2 += __shfl_xor(n2, 32);
            n2 = fmaxf(n2, __shfl_xor(n2, 1)); n2 = fmaxf(n2, __shfl_xor(n2, 2)); n2 = fmaxf(n2, __shfl_xor(n2, 4)); n2 = fmaxf(n2, __shfl_xor(n2, 8));
            if (lane == 0) kmaxp[tok0 >> 4] = n2;
        }
    }
    TS(0, 5);
}

constexpr int K2_K = 0, K2_V = 65536, K2_C = 131072, K2_Q = 135168, K2_KM = 143488, K2_ST = 144000, K2_RED = 158336, K2_LDS = 158848;
constexpr int K2_OSTR = 1040;
struct AttnState { f32x16 sA, sB, O; bf16x8 kf, va0, va1, vb0, vb1; lds_cptr kp, vp; };
#define ATT_STEP(SC, SN, V0, V1, W0, W1, KOFF, VOFF) do { \
        SN = mfma32(kf, qf, cinit); PIN(SN); \
        kf = ldsfrag(kp + (KOFF)); W0 = ldsfrag(vp + (VOFF)); W1 = ldsfrag(vp + (VOFF) + 512); \
        SBAR(); \
        u32x4 p0, p1; \
        _Pragma("unroll") for (int i = 0; i < 4; ++i) p0[i] = pack_bf16(EX(SC[2 * i]), EX(SC[2 * i + 1])); \
        SBAR(); \
        O = mfma32(V0, __builtin_bit_cast(bf16x8, p0), O); PIN(O); \
        SBAR(); \
        _Pragma("unroll") for (int i = 0; i < 4; ++i) p1[i] = pack_bf16(EX(SC[8 + 2 * i]), EX(SC[8 + 2 * i + 1])); \
        SBAR(); \
        O = mfma32(V1, __builtin_bit_cast(bf16x8, p1), O); PIN(O); \
        SBAR(); \
    } while (0)
#define ATT_RUN(N4) for (int it = 0; it < (N4); ++it) { \
        ATT_STEP(sA, sB, va0, va1, vb0, vb1, 2048, 1024); \
        ATT_STEP(sB, sA, vb0, vb1, va0, va1, 3072, 2048); \
        ATT_STEP(sA, sB, va0, va1, vb0, vb1, 4096, 3072); \
        vp += vinc; \
        ATT_STEP(sB, sA, vb0, vb1, va0, va1, 5120, 0); \
        kp += 4096; }
#define ATT_INIT() do { kp = L + K2_K + lane * 16; vp = vp0; O = f32x16{}; \
        kf = ldsfrag(kp); va0 = ldsfrag(vp); va1 = ldsfrag(vp + 512); sA = mfma32(kf, qf, cinit); kf = ldsfrag(kp + 1024); SBAR(); } while (0)
__global__ __launch_bounds__(512, 2) void k2_attn(const u16* __restrict__ Qf, const u16* __restrict__ Kf, const u16* __restrict__ Vf,
                                               const float* __restrict__ kmaxp, const u16* __restrict__ WoT, const u16* __restrict__ x,
                                               const u16* __restrict__ wg, const u16* __restrict__ wu,
                                               u16* __restrict__ hbuf, u16* __restrict__ HN, u16* __restrict__ WguT) {
    extern __shared__ __attribute__((aligned(16))) char smem[];
    const unsigned lds0 = (unsigned)(uintptr_t)smem;
    lds_ptr L = (lds_ptr)smem;
    const int tid = threadIdx.x, lane = tid & 63, w = (int)rfl(tid >> 6);
    const int rg = blockIdx.x;
    const int r = lane & 31, h = lane >> 5;
    TS(1, 0);
    glds16(Qf + (size_t)rg * 4096 + w * 512 + lane * 8, lds0 + K2_Q + w * 1024);
    if (lane < 32) glds16(kmaxp + lane * 4, lds0 + K2_KM);
#pragma unroll
    for (int n = 0; n < 5; ++n) {
        const int c = w * 5 + n;
        glds16((c < 20 ? Kf + (size_t)c * 512 : Vf + (size_t)(c - 20) * 512) + lane * 8, lds0 + (c < 20 ? K2_K + c * 1024 : K2_V + (c - 20) * 1024));
    }
#pragma unroll
    for (int n = 0; n < 11; ++n) {
        const int c = w * 11 + n;
        glds16((c < 44 ? Kf + (size_t)(20 + c) * 512 : Vf + (size_t)(c - 24) * 512) + lane * 8, lds0 + (c < 44 ? K2_K + (20 + c) * 1024 : K2_V + (c - 24) * 1024));
    }
    const int sp = rg & 15, jb = rg >> 4;
    if (w < 7) {
#pragma unroll
        for (int n = 0; n < 2; ++n) {
            const int c = w * 64 + lane, row = c / 14, ch = c - row * 14;
            glds16((n ? wu : wg) + (size_t)(32 * sp + row) * DF + 112 * jb + 8 * ch, lds0 + K2_ST + n * 7168 + w * 1024);
        }
    }
    if (tid < 256) {
        const int b = tid >> 5, sl = tid & 31;
        const unsigned val = sl == 0 ? 0x3f803f80u : 0u;
        *(LDSP u32x4*)(L + K2_C + b * 512 + sl * 16) = u32x4{val, val, val, val};
    }
    WAIT_VM_BAR(11);
    TS(1, 1);
    const bf16x8 qf = ldsfrag(L + K2_Q + w * 1024 + lane * 16);
    float m;
    {
        float km = fmaxf(*(LDSP const float*)(L + K2_KM + lane * 4), *(LDSP const float*)(L + K2_KM + 256 + lane * 4));
#pragma unroll
        for (int mm = 1; mm < 64; mm <<= 1) km = fmaxf(km, __shfl_xor(km, mm));
        float q2 = 0.f;
        const u32x4 qu = __builtin_bit_cast(u32x4, qf);
#pragma unroll
        for (int e = 0; e < 4; ++e) { float a = bf_lo(qu[e]), b = bf_hi(qu[e]); q2 += a * a + b * b; }
        q2 += __shfl_xor(q2, 32);
        m = sqrtf(q2 * km) * 1.001f + 1e-6f;
    }
    f32x16 cinit;
#pragma unroll
    for (int i = 0; i < 16; ++i) cinit[i] = -m;
    const bool isv = r < 16;
    const int cslot = (r == 16) ? 0 : ((r >= 20 && r < 28) ? 64 : 16);
    const lds_cptr vp0 = isv ? (L + K2_V + h * 256 + r * 16) : (L + K2_C + cslot);
    const int vinc = isv ? 4096 : 0;
    f32x16 sA, sB, O; bf16x8 kf, va0, va1, vb0, vb1; lds_cptr kp, vp;
    ATT_INIT();
    ATT_RUN(4);
    TS(1, 2);
    WAIT_VM_BAR(0);
    const int blk = (lane >> 4) & 1, q4 = (lane & 15) >> 2, p4 = lane & 3;
    if (w < 7) {
#pragma unroll
        for (int s2 = 0; s2 < 2; ++s2) {
            lds_cptr p = L + K2_ST + blk * 7168 + (16 * s2 + 8 * h + q4) * 224 + 32 * w + 8 * p4;
            bf16x8 f = trfrag(p, p + 4 * 224);
            *(bf16x8*)(WguT + ((size_t)(7 * jb + w) * 32 + 2 * sp + s2) * 512 + lane * 8) = f;
        }
    }
    const int c16 = lane & 15, g4 = lane >> 4;
    bf16x8 wf[2][16];
    u32x2 xr[4];
    {
        const u16* wp = WoT + (size_t)(4 * w) * 16 * 512 + lane * 8;
#pragma unroll
        for (int i = 0; i < 8; ++i) {
            const u16* p = wp + i * 2048;
            GLD16(wf[i >> 2][4 * (i & 3)], p, 0); GLD16(wf[i >> 2][4 * (i & 3) + 1], p, 1024); GLD16(wf[i >> 2][4 * (i & 3) + 2], p, 2048); GLD16(wf[i >> 2][4 * (i & 3) + 3], p, 3072);
        }
        const u16* xp = x + (size_t)(rg * 8 + (c16 & 7)) * D + 64 * w + 4 * g4;
        GLD8(xr[0], xp, 0); GLD8(xr[1], xp, 32); GLD8(xr[2], xp, 64); GLD8(xr[3], xp, 96);
    }
    TS(1, 3);
    ATT_RUN(12);
    TS(1, 4);
    float l = __shfl(O[8], r);
    if (__any(!(l >= 5.9604645e-8f))) {
        float mx = -INFINITY;
        const f32x16 z = {};
        for (int kb = 0; kb < 64; ++kb) {
            bf16x8 kq = ldsfrag(L + K2_K + kb * 1024 + lane * 16);
            f32x16 s = mfma32(kq, qf, z);
#pragma unroll
            for (int i = 0; i < 16; ++i) mx = fmaxf(mx, s[i]);
        }
        mx = fmaxf(mx, __shfl_xor(mx, 32));
#pragma unroll
        for (int i = 0; i < 16; ++i) cinit[i] = -mx;
        ATT_INIT();
        ATT_RUN(16);
        l = __shfl(O[8], r);
    }
    bf16x8 wa[16], wb[16];
    {
        const u16* wp = WoT + (size_t)(4 * w + 2) * 16 * 512 + lane * 8;
#pragma unroll
        for (int s = 0; s < 16; ++s) wa[s] = *(const bf16x8*)(wp + s * 512);
    }
    {
        const float inv = 1.f / l;
        lds_ptr dst = L + K2_Q + (r & 7) * K2_OSTR + ((4 * w + (r >> 3)) * 16 + 4 * h) * 2;
        u32x2 a, b;
        a[0] = pack_bf16(O[0] * inv, O[1] * inv); a[1] = pack_bf16(O[2] * inv, O[3] * inv);
        b[0] = pack_bf16(O[4] * inv, O[5] * inv); b[1] = pack_bf16(O[6] * inv, O[7] * inv);
        *(LDSP u32x2*)dst = a; *(LDSP u32x2*)(dst + 16) = b;
    }
    asm volatile("s_waitcnt vmcnt(16) lgkmcnt(0)\n\ts_barrier" ::: "memory");
    TS(1, 5);
#pragma unroll
    for (int i = 0; i < 16; ++i) { PIN(wf[0][i]); PIN(wf[1][i]); }
#pragma unroll
    for (int i = 0; i < 4; ++i) PIN(xr[i]);
    f32x4 acc[4] = {};
    lds_cptr ob = L + K2_Q + (c16 & 7) * K2_OSTR + g4 * 16;
#pragma unroll
    for (int s = 0; s < 16; ++s) {
        const bf16x8 b = ldsfrag(ob + s * 64);
        acc[0] = mfma16(wf[0][s], b, acc[0]);
        acc[1] = mfma16(wf[1][s], b, acc[1]);
    }
    {
        const u16* wp = WoT + (size_t)(4 * w + 3) * 16 * 512 + lane * 8;
#pragma unroll
        for (int s = 0; s < 16; ++s) wb[s] = *(const bf16x8*)(wp + s * 512);
    }
#pragma unroll
    for (int s = 0; s < 16; ++s) { const bf16x8 b = ldsfrag(ob + s * 64); acc[2] = mfma16(wa[s], b, acc[2]); }
#pragma unroll
    for (int s = 0; s < 16; ++s) { const bf16x8 b = ldsfrag(ob + s * 64); acc[3] = mfma16(wb[s], b, acc[3]); }
    PIN(acc[3]);
    TS(1, 6);
    u32x2 hv[4];
    float hf[16];
#pragma unroll
    for (int t = 0; t < 4; ++t) {
        hv[t][0] = pack_bf16(acc[t][0] + bf_lo(xr[t][0]), acc[t][1] + bf_hi(xr[t][0]));
        hv[t][1] = pack_bf16(acc[t][2] + bf_lo(xr[t][1]), acc[t][3] + bf_hi(xr[t][1]));
        hf[4 * t] = bf_lo(hv[t][0]); hf[4 * t + 1] = bf_hi(hv[t][0]); hf[4 * t + 2] = bf_lo(hv[t][1]); hf[4 * t + 3] = bf_hi(hv[t][1]);
    }
    float s1 = 0.f;
#pragma unroll
    for (int i = 0; i < 16; ++i) s1 += hf[i];
    s1 += __shfl_xor(s1, 16); s1 += __shfl_xor(s1, 32);
    const float mw = s1 * (1.f / 64.f);
    float m2 = 0.f;
#pragma unroll
    for (int i = 0; i < 16; ++i) { const float d = hf[i] - mw; m2 += d * d; }
    m2 += __shfl_xor(m2, 16); m2 += __shfl_xor(m2, 32);
    if (lane < 8) *(LDSP f32x2*)(L + K2_RED + (w * 8 + lane) * 8) = f32x2{mw, m2};
    const int row = rg * 8 + (c16 & 7);
    if (c16 < 8) {
#pragma unroll
        for (int t = 0; t < 4; ++t) *(u32x2*)(hbuf + (size_t)row * D + 64 * w + 16 * t + 4 * g4) = hv[t];
    }
    __syncthreads();
    float mean = 0.f, M2 = 0.f;
    {
        f32x2 st[8];
#pragma unroll
        for (int ww = 0; ww < 8; ++ww) { st[ww] = *(LDSP const f32x2*)(L + K2_RED + (ww * 8 + (c16 & 7)) * 8); mean += st[ww][0]; }
        mean *= 0.125f;
#pragma unroll
        for (int ww = 0; ww < 8; ++ww) { const float d = st[ww][0] - mean; M2 += st[ww][1] + 64.f * d * d; }
    }
    const float rstd = 1.f / sqrtf(M2 * (1.f / D) + LN_EPS);
    if (c16 < 8) {
#pragma unroll
        for (int t = 0; t < 4; ++t) {
            u32x2 o;
            o[0] = pack_bf16((hf[4 * t] - mean) * rstd, (hf[4 * t + 1] - mean) * rstd);
            o[1] = pack_bf16((hf[4 * t + 2] - mean) * rstd, (hf[4 * t + 3] - mean) * rstd);
            *(u32x2*)(HN + (((size_t)(row >> 5) * 32 + 4 * w + t) * 64 + (g4 >> 1) * 32 + (row & 31)) * 8 + 4 * (g4 & 1)) = o;
        }
    }
    TS(1, 7);
}

constexpr int K4_LDS = 163840;
DI float gelu_tanh(float g) {
    constexpr float c0 = -2.f * 0.7978845608028654f * 1.4426950408889634f, c1 = c0 * 0.044715f;
    const float e = __builtin_amdgcn_exp2f(g * __builtin_fmaf(g * g, c1, c0));
    return g * __builtin_amdgcn_rcpf(1.f + e);
}
__global__ __launch_bounds__(256, 2) void k4_gateup(const u16* __restrict__ HN, const u16* __restrict__ WguT, u16* __restrict__ HH) {
    extern __shared__ __attribute__((aligned(16))) char smem[];
    const unsigned lds0 = (unsigned)(uintptr_t)smem;
    lds_ptr L = (lds_ptr)smem;
    const int tid = threadIdx.x, lane = tid & 63, w = (int)rfl(tid >> 6);
    const int bid = blockIdx.x, xcd = bid & 7, idx = bid >> 3;
    const int ib = (xcd >> 2) * 8 + (idx >> 2), jb = (xcd & 3) * 4 + (idx & 3);
    const int tb = 4 * ib + w;
    TS(2, 0);
    const u16* wsrc = WguT + (size_t)(7 * jb) * 32 * 512 + (size_t)w * 8 * 512 + lane * 8;
    auto issue_tile = [&](int t, int slot) {
        glds16x4(wsrc + (size_t)t * 32 * 512, lds0 + slot * 32768 + w * 8192);
        glds16x4(wsrc + ((size_t)t * 32 + 4) * 512, lds0 + slot * 32768 + w * 8192 + 4096);
    };
    issue_tile(0, 0);
    bf16x8 xf[32];
    {
        const u16* hp = HN + (size_t)tb * 32 * 512 + lane * 8;
#pragma unroll
        for (int i = 0; i < 8; ++i) {
            const u16* p = hp + i * 2048;
            GLD16(xf[4 * i], p, 0); GLD16(xf[4 * i + 1], p, 1024); GLD16(xf[4 * i + 2], p, 2048); GLD16(xf[4 * i + 3], p, 3072);
        }
    }
    issue_tile(1, 1); issue_tile(2, 2);
    WAIT_VM_BAR(16);
    TS(2, 1);
#pragma unroll
    for (int i = 0; i < 32; ++i) PIN(xf[i]);
    issue_tile(3, 3); issue_tile(4, 4);
    f32x16 accp = {};
#pragma unroll
    for (int t = 0; t < 8; ++t) {
        const int slot = t % 5;
        lds_cptr wb = L + slot * 32768 + lane * 16;
        f32x16 acc = {};
        bf16x8 fa[2][4];
        float v[8];
        if (t < 7) {
#pragma unroll
            for (int i = 0; i < 4; ++i) fa[0][i] = ldsfrag(wb + i * 1024);
        }
#pragma unroll
        for (int b = 0; b < 8; ++b) {
            if (t < 7) {
                if (b < 7) {
#pragma unroll
                    for (int i = 0; i < 4; ++i) fa[(b + 1) & 1][i] = ldsfrag(wb + (4 * (b + 1) + i) * 1024);
                }
                SBAR();
#pragma unroll
                for (int i = 0; i < 4; ++i) acc = mfma32(fa[b & 1][i], xf[4 * b + i], acc);
                PIN(acc);
            }
            if (t > 0) { v[b] = gelu_tanh(accp[b]) * accp[b + 8]; PIN(v[b]); }
            SBAR();
        }
        if (t > 0) {
            u32x4 o;
            o[0] = pack_bf16(v[0], v[1]); o[1] = pack_bf16(v[2], v[3]); o[2] = pack_bf16(v[4], v[5]); o[3] = pack_bf16(v[6], v[7]);
            *(u32x4*)(HH + ((size_t)tb * 112 + 7 * jb + t - 1) * 512 + lane * 8) = o;
        }
        accp = acc;
        if (t < 3) TS(2, 2 + 2 * t);
        if (t == 0) { WAIT_VM_BAR(24); issue_tile(5, 0); }
        else if (t == 1) { WAIT_VM_BAR(25); issue_tile(6, 1); }
        else if (t == 2) { WAIT_VM_BAR(26); }
        else if (t == 3) { WAIT_VM_BAR(19); }
        else if (t == 4) { WAIT_VM_BAR(12); }
        else if (t == 5) { WAIT_VM_BAR(4); }
        if (t < 3) TS(2, 3 + 2 * t);
    }
}

constexpr int K5_LDS = 65536;
__global__ __launch_bounds__(256, 2) void k5_down(const u16* __restrict__ HH, const u16* __restrict__ WdT, const u16* __restrict__ hbuf, u16* __restrict__ out) {
    extern __shared__ __attribute__((aligned(16))) char smem[];
    lds_ptr L = (lds_ptr)smem;
    const int tid = threadIdx.x, lane = tid & 63, w = (int)rfl(tid >> 6);
    const int bid = blockIdx.x, xcd = bid & 7, idx = bid >> 3, ib = xcd * 4 + (idx >> 3), j = idx & 7;
    const u16* a0 = WdT + ((size_t)(2 * j) * 112 + 28 * w) * 512 + lane * 8;
    const u16* a1 = a0 + (size_t)112 * 512;
    const u16* b0 = HH + ((size_t)(2 * ib) * 112 + 28 * w) * 512 + lane * 8;
    const u16* b1 = b0 + (size_t)112 * 512;
    constexpr int P = 7;
    TS(3, 0);
    bf16x8 fr[P][4];
    f32x16 acc[2][2] = {};
#pragma unroll
    for (int s = 0; s < P; ++s) {
        fr[s][0] = *(const bf16x8*)(a0 + s * 512); fr[s][1] = *(const bf16x8*)(a1 + s * 512);
        fr[s][2] = *(const bf16x8*)(b0 + s * 512); fr[s][3] = *(const bf16x8*)(b1 + s * 512);
    }
    SBAR();
#pragma unroll
    for (int s = 0; s < 28; ++s) {
        const int sl = s % P;
        acc[0][0] = mfma32(fr[sl][0], fr[sl][2], acc[0][0]);
        acc[0][1] = mfma32(fr[sl][0], fr[sl][3], acc[0][1]);
        acc[1][0] = mfma32(fr[sl][1], fr[sl][2], acc[1][0]);
        acc[1][1] = mfma32(fr[sl][1], fr[sl][3], acc[1][1]);
        PIN(acc[0][0]); PIN(acc[0][1]); PIN(acc[1][0]); PIN(acc[1][1]);
        SBAR();
        if (s + P < 28) {
            fr[sl][0] = *(const bf16x8*)(a0 + (s + P) * 512); fr[sl][1] = *(const bf16x8*)(a1 + (s + P) * 512);
            fr[sl][2] = *(const bf16x8*)(b0 + (s + P) * 512); fr[sl][3] = *(const bf16x8*)(b1 + (s + P) * 512);
        }
        SBAR();
    }
    TS(3, 1);
    const int r = lane & 31, h = lane >> 5;
#pragma unroll
    for (int q = 0; q < 4; ++q) {
        if (q != w) {
            const int slot = (w - q + 3) & 3;
#pragma unroll
            for (int g = 0; g < 4; ++g) {
                f32x4 v = {acc[q >> 1][q & 1][4 * g], acc[q >> 1][q & 1][4 * g + 1], acc[q >> 1][q & 1][4 * g + 2], acc[q >> 1][q & 1][4 * g + 3]};
                *(LDSP f32x4*)(L + ((q * 3 + slot) * 4 + g) * 1024 + lane * 16) = v;
            }
        }
    }
    const int na = w >> 1, tbq = w & 1;
    const size_t base = (size_t)(ib * 64 + 32 * tbq + r) * D + 64 * j + 32 * na + 4 * h;
    u32x2 xv[4];
#pragma unroll
    for (int g = 0; g < 4; ++g) xv[g] = *(const u32x2*)(hbuf + base + 8 * g);
    __syncthreads();
    TS(3, 2);
    f32x16 t = (w == 0) ? acc[0][0] : (w == 1) ? acc[0][1] : (w == 2) ? acc[1][0] : acc[1][1];
#pragma unroll
    for (int slot = 0; slot < 3; ++slot)
#pragma unroll
        for (int g = 0; g < 4; ++g) {
            const f32x4 v = *(LDSP const f32x4*)(L + ((w * 3 + slot) * 4 + g) * 1024 + lane * 16);
            t[4 * g] += v[0]; t[4 * g + 1] += v[1]; t[4 * g + 2] += v[2]; t[4 * g + 3] += v[3];
        }
#pragma unroll
    for (int g = 0; g < 4; ++g) {
        u32x2 ov;
        ov[0] = pack_bf16(t[4 * g] + bf_lo(xv[g][0]), t[4 * g + 1] + bf_hi(xv[g][0]));
        ov[1] = pack_bf16(t[4 * g + 2] + bf_lo(xv[g][1]), t[4 * g + 3] + bf_hi(xv[g][1]));
        *(u32x2*)(out + base + 8 * g) = ov;
    }
    TS(3, 3);
}

extern "C" void kernel_launch(void* const* d_in, const int* in_sizes, int n_in, void* d_out, int out_size, void* d_ws, size_t ws_size,
                              hipStream_t stream) {
    const u16* x = (const u16*)d_in[0];
    const u16* wq = (const u16*)d_in[1];
    const u16* wk = (const u16*)d_in[2];
    const u16* wv = (const u16*)d_in[3];
    const u16* wo = (const u16*)d_in[4];
    const u16* wg = (const u16*)d_in[6];
    const u16* wu = (const u16*)d_in[7];
    const u16* wd = (const u16*)d_in[8];
    char* ws = (char*)d_ws;
    u16* Qf = (u16*)(ws);
    u16* Kf = (u16*)(ws + (2 << 20));
    u16* Vf = (u16*)(ws + (2 << 20) + 65536);
    float* kmaxp = (float*)(ws + (2 << 20) + 131072);
    u16* WoT = (u16*)(ws + (3 << 20));
    u16* HN = (u16*)(ws + (4 << 20));
    u16* hbuf = (u16*)(ws + (6 << 20));
    u16* HH = (u16*)(ws + (8 << 20));
    u16* WguT = (u16*)(ws + (16 << 20));
    u16* WdT = (u16*)(ws + (20 << 20));
    (void)hipFuncSetAttribute((const void*)k1_ln_qkv, hipFuncAttributeMaxDynamicSharedMemorySize, K1_LDS);
    (void)hipFuncSetAttribute((const void*)k2_attn, hipFuncAttributeMaxDynamicSharedMemorySize, K2_LDS);
    (void)hipFuncSetAttribute((const void*)k4_gateup, hipFuncAttributeMaxDynamicSharedMemorySize, K4_LDS);
    (void)hipFuncSetAttribute((const void*)k5_down, hipFuncAttributeMaxDynamicSharedMemorySize, K5_LDS);
    k1_ln_qkv<<<256, 512, K1_LDS, stream>>>(x, wq, wk, wv, wo, wd, Qf, Kf, Vf, kmaxp, WoT, WdT);
    k2_attn<<<256, 512, K2_LDS, stream>>>(Qf, Kf, Vf, kmaxp, WoT, x, wg, wu, hbuf, HN, WguT);
    k4_gateup<<<256, 256, K4_LDS, stream>>>(HN, WguT, HH);
    k5_down<<<256, 256, K5_LDS, stream>>>(HH, WdT, hbuf, (u16*)d_out);
}
```

```cpp
#include <hip/hip_runtime.h>
#include <hip/hip_bf16.h>
#include <stdint.h>

typedef unsigned short u16;
typedef __attribute__((ext_vector_type(8))) short bf16x8;
typedef __attribute__((ext_vector_type(4))) short s16x4;
typedef __attribute__((ext_vector_type(16))) float f32x16;
typedef __attribute__((ext_vector_type(4))) float f32x4;
typedef __attribute__((ext_vector_type(2))) float f32x2;
typedef __attribute__((ext_vector_type(4))) unsigned u32x4;
typedef __attribute__((ext_vector_type(2))) unsigned u32x2;
typedef __attribute__((ext_vector_type(2))) __bf16 bf16x2_t;

#define DI __device__ __forceinline__
#define LDSP __attribute__((address_space(3)))
typedef LDSP const char* lds_cptr;
typedef LDSP char* lds_ptr;

constexpr int S = 2048, D = 512, DF = 1792;
constexpr float LN_EPS = 1e-5f;
constexpr float QSCALE = 0.25f * 1.4426950408889634f;

DI unsigned pack_bf16(float a, float b) { f32x2 v = {a, b}; bf16x2_t r = __builtin_convertvector(v, bf16x2_t); return __builtin_bit_cast(unsigned, r); }
DI float bf_lo(unsigned u) { return __uint_as_float(u << 16); }
DI float bf_hi(unsigned u) { return __uint_as_float(u & 0xffff0000u); }
DI f32x16 mfma32(bf16x8 a, bf16x8 b, f32x16 c) { return __builtin_amdgcn_mfma_f32_32x32x16_bf16(a, b, c, 0, 0, 0); }
DI f32x4 mfma16(bf16x8 a, bf16x8 b, f32x4 c) { return __builtin_amdgcn_mfma_f32_16x16x32_bf16(a, b, c, 0, 0, 0); }
DI void glds16(const void* g, unsigned lds_base) {
    unsigned sv;
    asm volatile("s_mov_b32 %0, m0\n\ts_mov_b32 m0, %2\n\ts_nop 0\n\tglobal_load_lds_dwordx4 %1, off\n\ts_mov_b32 m0, %0"
                 : "=&s"(sv) : "v"(g), "s"(lds_base) : "memory");
}
DI void glds16x4(const void* g, unsigned lds_base) {
    unsigned sv;
    asm volatile("s_mov_b32 %0, m0\n\ts_mov_b32 m0, %2\n\ts_nop 0\n\tglobal_load_lds_dwordx4 %1, off\n\tglobal_load_lds_dwordx4 %1, off offset:1024\n\t"
                 "global_load_lds_dwordx4 %1, off offset:2048\n\tglobal_load_lds_dwordx4 %1, off offset:3072\n\ts_mov_b32 m0, %0"
                 : "=&s"(sv) : "v"(g), "s"(lds_base) : "memory");
}
template <int N> DI void glds_run(const u16* g, unsigned lds_base) {
#pragma unroll
    for (int i = 0; i + 4 <= N; i += 4) glds16x4(g + i * 512, lds_base + i * 1024);
#pragma unroll
    for (int i = N & ~3; i < N; ++i) glds16(g + i * 512, lds_base + i * 1024);
}
DI unsigned rfl(unsigned v) { return (unsigned)__builtin_amdgcn_readfirstlane((int)v); }
DI s16x4 trread(lds_cptr p) { return __builtin_amdgcn_ds_read_tr16_b64_v4i16((LDSP s16x4*)p); }
DI bf16x8 trfrag(lds_cptr p0, lds_cptr p1) { s16x4 a = trread(p0), b = trread(p1); return __builtin_shufflevector(a, b, 0, 1, 2, 3, 4, 5, 6, 7); }
DI bf16x8 ldsfrag(lds_cptr p) { return *(LDSP const bf16x8*)p; }
#define SBAR() __builtin_amdgcn_sched_barrier(0)
#define PIN(x) asm volatile("" : "+v"(x))
#define EX(x) __builtin_amdgcn_exp2f(x)
#define GLD16(dst, ptr, imm) asm volatile("global_load_dwordx4 %0, %1, off offset:%2" : "=v"(dst) : "v"(ptr), "i"(imm))
#define GLD8(dst, ptr, imm) asm volatile("global_load_dwordx2 %0, %1, off offset:%2" : "=v"(dst) : "v"(ptr), "i"(imm))
#define K5P 14
#define ABL4 0
#define TS(k, i)
#define WAIT_VM(N) asm volatile("s_waitcnt vmcnt(" #N ")" ::: "memory")
#define WAIT_VM_BAR(N) asm volatile("s_waitcnt vmcnt(" #N ") lgkmcnt(0)\n\ts_barrier" ::: "memory")

DI void ln_rows8(const u32x4 (&v)[8], int lane, lds_ptr tile, int row0) {
    const int rr = lane >> 3, cc = lane & 7;
    f32x2 f[32];
#pragma unroll
    for (int c = 0; c < 8; ++c)
#pragma unroll
        for (int e = 0; e < 4; ++e) f[4 * c + e] = f32x2{bf_lo(v[c][e]), bf_hi(v[c][e])};
    f32x2 s2 = f[0];
#pragma unroll
    for (int i = 1; i < 32; ++i) s2 += f[i];
    float s = s2[0] + s2[1];
    s += __shfl_xor(s, 1); s += __shfl_xor(s, 2); s += __shfl_xor(s, 4);
    const float mean = s * (1.f / D);
    const f32x2 m2 = {mean, mean};
    f32x2 q2 = {0.f, 0.f};
#pragma unroll
    for (int i = 0; i < 32; ++i) { f[i] -= m2; q2 += f[i] * f[i]; }
    float q = q2[0] + q2[1];
    q += __shfl_xor(q, 1); q += __shfl_xor(q, 2); q += __shfl_xor(q, 4);
    const float rstd = 1.f / sqrtf(q * (1.f / D) + LN_EPS);
    const f32x2 r2 = {rstd, rstd};
    const int row = row0 + rr;
#pragma unroll
    for (int c = 0; c < 8; ++c) {
        u32x4 o;
#pragma unroll
        for (int e = 0; e < 4; ++e) { const f32x2 t = f[4 * c + e] * r2; o[e] = pack_bf16(t[0], t[1]); }
        *(LDSP u32x4*)(tile + row * 1024 + (((cc + 8 * c) ^ (row & 15)) << 4)) = o;
    }
}

constexpr int K1_XN = 0, K1_WQ = 65536, K1_WKV = 131072, K1_ST = 147456, K1_LDS = K1_ST + 9216;
__global__ __launch_bounds__(512, 2) void k1_ln_qkv(const u16* __restrict__ x, const u16* __restrict__ wq, const u16* __restrict__ wk,
                                                  const u16* __restrict__ wv, const u16* __restrict__ wo, const u16* __restrict__ wd,
                                                  u16* __restrict__ Qf, u16* __restrict__ Kf, u16* __restrict__ Vf, float* __restrict__ kmaxp,
                                                  u16* __restrict__ WoT, u16* __restrict__ WdT) {
    extern __shared__ __attribute__((aligned(16))) char smem[];
    const unsigned lds0 = (unsigned)(uintptr_t)smem;
    lds_ptr L = (lds_ptr)smem;
    const int tid = threadIdx.x, lane = tid & 63, w = (int)rfl(tid >> 6);
    const int bid = blockIdx.x, xcd = bid & 7, idx = bid >> 3, ib = xcd * 4 + (idx >> 3), j = idx & 7;
    const int row0 = ib * 64;
    TS(0, 0);
    u32x4 xv[8];
    {
        const u16* p = x + (size_t)(row0 + 8 * w + (lane >> 3)) * D + 8 * (lane & 7);
        GLD16(xv[0], p, 0); GLD16(xv[1], p, 128); GLD16(xv[2], p, 256); GLD16(xv[3], p, 384);
        GLD16(xv[4], p, 512); GLD16(xv[5], p, 640); GLD16(xv[6], p, 768); GLD16(xv[7], p, 896);
    }
    {
        const u16* src = wq + (size_t)((lane >> 2) & 7) * D + 64 * j + 32 * (lane >> 5) + 8 * (lane & 3);
#pragma unroll
        for (int n = 0; n < 8; ++n) { const int nn = w * 8 + n; glds16(src + (size_t)nn * 8 * D, lds0 + K1_WQ + nn * 1024); }
        const u16* s2 = ((j & 1) ? wv : wk) + lane * 8;
#pragma unroll
        for (int n = 0; n < 2; ++n) { const int nn = w * 2 + n; glds16(s2 + nn * 512, lds0 + K1_WKV + nn * 1024); }
        const int nt = bid & 15, sg = bid >> 4;
        {
            const u16* p = w < 7 ? wd + (size_t)(112 * sg + 16 * w + (lane >> 2)) * D + 32 * nt + 8 * (lane & 3)
                                 : wo + (size_t)(32 * sg + (lane >> 2)) * D + 32 * nt + 8 * (lane & 3);
            glds16(p, lds0 + K1_ST + w * 1024);
            if (w == 7) glds16(p + (size_t)16 * D, lds0 + K1_ST + 8 * 1024);
        }
    }
    TS(0, 1);
    WAIT_VM(11);
#pragma unroll
    for (int c = 0; c < 8; ++c) PIN(xv[c]);
    ln_rows8(xv, lane, L + K1_XN, 8 * w);
    TS(0, 2);
    WAIT_VM_BAR(0);
    TS(0, 3);
    const int r = lane & 31, h = lane >> 5, blk = (lane >> 4) & 1, q4 = (lane & 15) >> 2, p4 = lane & 3;
    const int wm = w & 1, wn = (w >> 1) & 1, kh = w >> 2;
    f32x16 acc = {};
    f32x4 akv = {};
    const int trow = 32 * wm + r;
    {
        lds_cptr wb = L + K1_WQ + kh * 32768 + h * 1024 + wn * 512 + q4 * 64 + 32 * blk + 8 * p4;
        lds_cptr xb = L + K1_XN + trow * 1024 + kh * 512;
        bf16x8 fa[16], fb[16];
#pragma unroll
        for (int i = 0; i < 16; ++i) { fa[i] = trfrag(wb + i * 2048, wb + i * 2048 + 256); fb[i] = ldsfrag(xb + ((((2 * i + h) & 15) ^ (trow & 15)) << 4) + (i >> 3) * 256); }
        const int g4 = lane >> 4, c16 = lane & 15, krow = 16 * (j >> 1) + c16;
        bf16x8 wf[2], xf[2];
#pragma unroll
        for (int ss = 0; ss < 2; ++ss) {
            const int s = 2 * w + ss;
            lds_cptr wp = L + K1_WKV + (32 * s + 8 * g4 + q4) * 32 + 8 * p4;
            wf[ss] = trfrag(wp, wp + 128);
            xf[ss] = ldsfrag(L + K1_XN + krow * 1024 + (((4 * s + g4) ^ (krow & 15)) << 4));
        }
#pragma unroll
        for (int i = 0; i < 16; ++i) acc = mfma32(fa[i], fb[i], acc);
#pragma unroll
        for (int ss = 0; ss < 2; ++ss) akv = (j & 1) ? mfma16(xf[ss], wf[ss], akv) : mfma16(wf[ss], xf[ss], akv);
    }
    TS(0, 4);
    {
        const int nt = bid & 15, sg = bid >> 4;
        if (w < 7) {
            lds_cptr p = L + K1_ST + w * 1024 + (4 * h + q4) * 64 + 32 * blk + 8 * p4;
            bf16x8 f = trfrag(p, p + 512);
            *(bf16x8*)(WdT + ((size_t)nt * 112 + 7 * sg + w) * 512 + lane * 8) = f;
        } else {
            const int g4 = lane >> 4;
#pragma unroll
            for (int n = 0; n < 2; ++n) {
                lds_cptr p = L + K1_ST + 7 * 1024 + (8 * g4 + q4) * 64 + 32 * n + 8 * p4;
                bf16x8 f = trfrag(p, p + 256);
                *(bf16x8*)(WoT + ((size_t)(2 * nt + n) * 16 + sg) * 512 + lane * 8) = f;
            }
        }
    }
    asm volatile("s_waitcnt lgkmcnt(0)\n\ts_barrier" ::: "memory");
    {
        LDSP f32x4* ex = (LDSP f32x4*)(L + K1_WQ + w * 2048 + lane * 16);
        const int o = kh ? 0 : 8;
        f32x4 e0, e1;
#pragma unroll
        for (int e = 0; e < 4; ++e) { e0[e] = kh ? acc[e] : acc[8 + e]; e1[e] = kh ? acc[4 + e] : acc[12 + e]; }
        (void)o;
        ex[0] = e0; ex[64] = e1;
        *(LDSP f32x4*)(L + K1_WQ + 16384 + w * 1024 + lane * 16) = akv;
    }
    __syncthreads();
    {
        LDSP const f32x4* ex = (LDSP const f32x4*)(L + K1_WQ + (w ^ 4) * 2048 + lane * 16);
        const f32x4 e0 = ex[0], e1 = ex[64];
        const int grow = row0 + trow;
#pragma unroll
        for (int gg = 0; gg < 2; ++gg) {
            const int g = 2 * kh + gg;
            float v[4];
#pragma unroll
            for (int e = 0; e < 4; ++e) v[e] = ((kh ? acc[8 + 4 * gg + e] : acc[4 * gg + e]) + (gg ? e1[e] : e0[e])) * QSCALE;
            const int head = (64 * j + 32 * wn) / 16 + (g >> 1);
            u32x2 o; o[0] = pack_bf16(v[0], v[1]); o[1] = pack_bf16(v[2], v[3]);
            u16* dst = Qf + ((((size_t)(grow >> 3) * 8 + (head >> 2)) * 2 + (g & 1)) * 32 + (head & 3) * 8 + (grow & 7)) * 8 + 4 * h;
            *(u32x2*)dst = o;
        }
    }
    if (w == 0) {
        const int g4 = lane >> 4, c16 = lane & 15;
        f32x4 t = akv;
#pragma unroll
        for (int ww = 1; ww < 8; ++ww) { f32x4 u = *(LDSP const f32x4*)(L + K1_WQ + 16384 + ww * 1024 + lane * 16); t += u; }
        u32x2 o; o[0] = pack_bf16(t[0], t[1]); o[1] = pack_bf16(t[2], t[3]);
        const int tok0 = row0 + 16 * (j >> 1);
        if (j & 1) {
            const int kb = tok0 >> 5, s = (tok0 >> 4) & 1;
            *(u32x2*)(Vf + ((((size_t)kb * 2 + s) * 2 + (g4 & 1)) * 16 + c16) * 8 + 4 * (g4 >> 1)) = o;
        } else {
            const int key = tok0 + c16;
            *(u32x2*)(Kf + (((size_t)(key >> 5) * 2 + (g4 >> 1)) * 32 + (key & 31)) * 8 + 4 * (g4 & 1)) = o;
            float a = bf_lo(o[0]), b = bf_hi(o[0]), c = bf_lo(o[1]), d = bf_hi(o[1]);
            float n2 = a * a + b * b + c * c + d * d;
            n2 += __shfl_xor(n2, 16); n2 += __shfl_xor(n2, 32);
            n2 = fmaxf(n2, __shfl_xor(n2, 1)); n2 = fmaxf(n2, __shfl_xor(n2, 2)); n2 = fmaxf(n2, __shfl_xor(n2, 4)); n2 = fmaxf(n2, __shfl_xor(n2, 8));
            if (lane == 0) kmaxp[tok0 >> 4] = n2;
        }
    }
    TS(0, 5);
}

constexpr int K2_K = 0, K2_V = 65536, K2_C = 131072, K2_Q = 135168, K2_KM = 143488, K2_ST = 144000, K2_RED = 158336, K2_LDS = 158848;
constexpr int K2_OSTR = 1040;
struct AttnState { f32x16 sA, sB, O; bf16x8 kf, va0, va1, vb0, vb1; lds_cptr kp, vp; };
#define ATT_STEP(SC, SN, V0, V1, W0, W1, KOFF, VOFF) do { \
        SN = mfma32(kf, qf, cinit); PIN(SN); \
        kf = ldsfrag(kp + (KOFF)); W0 = ldsfrag(vp + (VOFF)); W1 = ldsfrag(vp + (VOFF) + 512); \
        SBAR(); \
        u32x4 p0, p1; \
        _Pragma("unroll") for (int i = 0; i < 4; ++i) p0[i] = pack_bf16(EX(SC[2 * i]), EX(SC[2 * i + 1])); \
        SBAR(); \
        O = mfma32(V0, __builtin_bit_cast(bf16x8, p0), O); PIN(O); \
        SBAR(); \
        _Pragma("unroll") for (int i = 0; i < 4; ++i) p1[i] = pack_bf16(EX(SC[8 + 2 * i]), EX(SC[8 + 2 * i + 1])); \
        SBAR(); \
        O = mfma32(V1, __builtin_bit_cast(bf16x8, p1), O); PIN(O); \
        SBAR(); \
    } while (0)
#define ATT_RUN(N4) for (int it = 0; it < (N4); ++it) { \
        ATT_STEP(sA, sB, va0, va1, vb0, vb1, 2048, 1024); \
        ATT_STEP(sB, sA, vb0, vb1, va0, va1, 3072, 2048); \
        ATT_STEP(sA, sB, va0, va1, vb0, vb1, 4096, 3072); \
        vp += vinc; \
        ATT_STEP(sB, sA, vb0, vb1, va0, va1, 5120, 0); \
        kp += 4096; }
#define ATT_INIT() do { kp = L + K2_K + lane * 16; vp = vp0; O = f32x16{}; \
        kf = ldsfrag(kp); va0 = ldsfrag(vp); va1 = ldsfrag(vp + 512); sA = mfma32(kf, qf, cinit); kf = ldsfrag(kp + 1024); SBAR(); } while (0)
__global__ __launch_bounds__(512, 2) void k2_attn(const u16* __restrict__ Qf, const u16* __restrict__ KVf,
                                               const float* __restrict__ kmaxp, const u16* __restrict__ WoT, const u16* __restrict__ x,
                                               const u16* __restrict__ wg, const u16* __restrict__ wu,
                                               float* __restrict__ hbuf, u16* __restrict__ HN, u16* __restrict__ WguT) {
    extern __shared__ __attribute__((aligned(16))) char smem[];
    const unsigned lds0 = (unsigned)(uintptr_t)smem;
    lds_ptr L = (lds_ptr)smem;
    const int tid = threadIdx.x, lane = tid & 63, w = (int)rfl(tid >> 6);
    const int rg = blockIdx.x;
    const int r = lane & 31, h = lane >> 5;
    TS(1, 0);
    glds16(Qf + (size_t)rg * 4096 + w * 512 + lane * 8, lds0 + K2_Q + w * 1024);
    if (lane < 32) glds16(kmaxp + lane * 4, lds0 + K2_KM);
    const int kv = (w >> 2) * 64, wq4 = w & 3;
    glds_run<5>(KVf + (size_t)(kv + 5 * wq4) * 512 + lane * 8, lds0 + K2_K + (kv + 5 * wq4) * 1024);
    const u16* kvrest = KVf + (size_t)(kv + 20 + 11 * wq4) * 512 + lane * 8;
    const unsigned kvdst = lds0 + K2_K + (kv + 20 + 11 * wq4) * 1024;
    const int sp = rg & 15, jb = rg >> 4;
    if (tid < 256) {
        const int b = tid >> 5, sl = tid & 31;
        const unsigned val = sl == 0 ? 0x3f803f80u : 0u;
        *(LDSP u32x4*)(L + K2_C + b * 512 + sl * 16) = u32x4{val, val, val, val};
    }
    WAIT_VM_BAR(0);
    TS(1, 1);
    glds_run<4>(kvrest, kvdst);
    const bf16x8 qf = ldsfrag(L + K2_Q + w * 1024 + lane * 16);
    float m;
    {
        float km = fmaxf(*(LDSP const float*)(L + K2_KM + lane * 4), *(LDSP const float*)(L + K2_KM + 256 + lane * 4));
#pragma unroll
        for (int mm = 1; mm < 64; mm <<= 1) km = fmaxf(km, __shfl_xor(km, mm));
        float q2 = 0.f;
        const u32x4 qu = __builtin_bit_cast(u32x4, qf);
#pragma unroll
        for (int e = 0; e < 4; ++e) { float a = bf_lo(qu[e]), b = bf_hi(qu[e]); q2 += a * a + b * b; }
        q2 += __shfl_xor(q2, 32);
        m = sqrtf(q2 * km) * 1.001f + 1e-6f;
    }
    f32x16 cinit;
#pragma unroll
    for (int i = 0; i < 16; ++i) cinit[i] = -m;
    const bool isv = r < 16;
    const int cslot = (r == 16) ? 0 : ((r >= 20 && r < 28) ? 64 : 16);
    const lds_cptr vp0 = isv ? (L + K2_V + h * 256 + r * 16) : (L + K2_C + cslot);
    const int vinc = isv ? 4096 : 0;
    f32x16 sA, sB, O; bf16x8 kf, va0, va1, vb0, vb1; lds_cptr kp, vp;
    ATT_INIT();
    ATT_RUN(1);
    glds_run<4>(kvrest + 4 * 512, kvdst + 4096);
    ATT_RUN(1);
    glds_run<3>(kvrest + 8 * 512, kvdst + 8192);
    ATT_RUN(1);
    if (w < 7) {
#pragma unroll
        for (int n = 0; n < 2; ++n) {
            const int c = w * 64 + lane, row = c / 14, ch = c - row * 14;
            glds16((n ? wu : wg) + (size_t)(32 * sp + row) * DF + 112 * jb + 8 * ch, lds0 + K2_ST + n * 7168 + w * 1024);
        }
    }
    ATT_RUN(1);
    TS(1, 2);
    WAIT_VM_BAR(0);
    const int blk = (lane >> 4) & 1, q4 = (lane & 15) >> 2, p4 = lane & 3;
    if (w < 7) {
#pragma unroll
        for (int s2 = 0; s2 < 2; ++s2) {
            lds_cptr p = L + K2_ST + blk * 7168 + (16 * s2 + 8 * h + q4) * 224 + 32 * w + 8 * p4;
            bf16x8 f = trfrag(p, p + 4 * 224);
            *(bf16x8*)(WguT + ((size_t)(7 * jb + w) * 32 + 2 * sp + s2) * 512 + lane * 8) = f;
        }
    }
    const int c16 = lane & 15, g4 = lane >> 4;
    bf16x8 wf[2][16];
    u32x2 xr[4];
    const u16* wp0 = WoT + (size_t)(4 * w) * 16 * 512 + lane * 8;
#define WO_BURST(i0) do { _Pragma("unroll") for (int i = (i0); i < (i0) + 2; ++i) { const u16* p = wp0 + i * 2048; \
        GLD16(wf[i >> 2][4 * (i & 3)], p, 0); GLD16(wf[i >> 2][4 * (i & 3) + 1], p, 1024); GLD16(wf[i >> 2][4 * (i & 3) + 2], p, 2048); GLD16(wf[i >> 2][4 * (i & 3) + 3], p, 3072); } } while (0)
    WO_BURST(0);
    TS(1, 3);
    ATT_RUN(3);
    WO_BURST(2);
    ATT_RUN(3);
    asm volatile("s_barrier" ::: "memory");
    glds_run<5>(wp0 + (size_t)32 * 512, lds0 + K2_K + 5 * w * 1024);
    glds_run<5>(wp0 + (size_t)37 * 512, lds0 + K2_V + 5 * w * 1024);
    WO_BURST(4);
    ATT_RUN(3);
    WO_BURST(6);
    {
        const u16* xp = x + (size_t)(rg * 8 + (c16 & 7)) * D + 64 * w + 4 * g4;
        GLD8(xr[0], xp, 0); GLD8(xr[1], xp, 32); GLD8(xr[2], xp, 64); GLD8(xr[3], xp, 96);
    }
    ATT_RUN(3);
    TS(1, 4);
    float l = __shfl(O[8], r);
    if (__any(!(l >= 5.9604645e-8f))) {
        asm volatile("s_waitcnt vmcnt(0)" ::: "memory");
        const u16* kg = KVf + lane * 8;
        const u16* vg = KVf + 64 * 512 + h * 128 + (r & 15) * 8;
        const unsigned cv = (r == 16) ? 0x3f803f80u : 0u;
        const u32x4 cst = {cv, cv, cv, cv};
        float mx = -INFINITY;
        const f32x16 z = {};
        for (int kb = 0; kb < 64; ++kb) {
            const f32x16 s = mfma32(*(const bf16x8*)(kg + kb * 512), qf, z);
#pragma unroll
            for (int i = 0; i < 16; ++i) mx = fmaxf(mx, s[i]);
        }
        mx = fmaxf(mx, __shfl_xor(mx, 32));
#pragma unroll
        for (int i = 0; i < 16; ++i) cinit[i] = -mx;
        O = f32x16{};
        for (int kb = 0; kb < 64; ++kb) {
            const f32x16 s = mfma32(*(const bf16x8*)(kg + kb * 512), qf, cinit);
            u32x4 v0 = *(const u32x4*)(vg + kb * 512), v1 = *(const u32x4*)(vg + kb * 512 + 256);
            if (!isv) { v0 = cst; v1 = cst; }
            u32x4 p0, p1;
#pragma unroll
            for (int i = 0; i < 4; ++i) { p0[i] = pack_bf16(EX(s[2 * i]), EX(s[2 * i + 1])); p1[i] = pack_bf16(EX(s[8 + 2 * i]), EX(s[8 + 2 * i + 1])); }
            O = mfma32(__builtin_bit_cast(bf16x8, v0), __builtin_bit_cast(bf16x8, p0), O);
            O = mfma32(__builtin_bit_cast(bf16x8, v1), __builtin_bit_cast(bf16x8, p1), O);
        }
        l = __shfl(O[8], r);
    }
    bf16x8 wl[22];
#pragma unroll
    for (int i = 0; i < 22; ++i) wl[i] = *(const bf16x8*)(wp0 + (size_t)(42 + i) * 512);
    {
        const float inv = 1.f / l;
        lds_ptr dst = L + K2_Q + (r & 7) * K2_OSTR + ((4 * w + (r >> 3)) * 16 + 4 * h) * 2;
        u32x2 a, b;
        a[0] = pack_bf16(O[0] * inv, O[1] * inv); a[1] = pack_bf16(O[2] * inv, O[3] * inv);
        b[0] = pack_bf16(O[4] * inv, O[5] * inv); b[1] = pack_bf16(O[6] * inv, O[7] * inv);
        *(LDSP u32x2*)dst = a; *(LDSP u32x2*)(dst + 16) = b;
    }
    asm volatile("s_waitcnt vmcnt(22) lgkmcnt(0)\n\ts_barrier" ::: "memory");
    TS(1, 5);
#pragma unroll
    for (int i = 0; i < 16; ++i) { PIN(wf[0][i]); PIN(wf[1][i]); }
#pragma unroll
    for (int i = 0; i < 4; ++i) PIN(xr[i]);
    f32x4 acc[4] = {};
    lds_cptr ob = L + K2_Q + (c16 & 7) * K2_OSTR + g4 * 16;
#pragma unroll
    for (int s = 0; s < 16; ++s) {
        const bf16x8 b = ldsfrag(ob + s * 64);
        acc[0] = mfma16(wf[0][s], b, acc[0]);
        acc[1] = mfma16(wf[1][s], b, acc[1]);
        const bf16x8 a2 = s < 10 ? ldsfrag(L + (s < 5 ? K2_K + (5 * w + s) * 1024 : K2_V + (5 * w + s - 5) * 1024) + lane * 16) : wl[s - 10];
        acc[2] = mfma16(a2, b, acc[2]);
        acc[3] = mfma16(wl[6 + s], b, acc[3]);
    }
    PIN(acc[3]);
    TS(1, 6);
    float hf[16];
#pragma unroll
    for (int t = 0; t < 4; ++t) {
        hf[4 * t] = acc[t][0] + bf_lo(xr[t][0]); hf[4 * t + 1] = acc[t][1] + bf_hi(xr[t][0]);
        hf[4 * t + 2] = acc[t][2] + bf_lo(xr[t][1]); hf[4 * t + 3] = acc[t][3] + bf_hi(xr[t][1]);
    }
    float s1 = 0.f;
#pragma unroll
    for (int i = 0; i < 16; ++i) s1 += hf[i];
    s1 += __shfl_xor(s1, 16); s1 += __shfl_xor(s1, 32);
    const float mw = s1 * (1.f / 64.f);
    float m2 = 0.f;
#pragma unroll
    for (int i = 0; i < 16; ++i) { const float d = hf[i] - mw; m2 += d * d; }
    m2 += __shfl_xor(m2, 16); m2 += __shfl_xor(m2, 32);
    if (lane < 8) *(LDSP f32x2*)(L + K2_RED + (w * 8 + lane) * 8) = f32x2{mw, m2};
    const int row = rg * 8 + (c16 & 7);
    asm volatile("s_waitcnt lgkmcnt(0)\n\ts_barrier" ::: "memory");
    float mean = 0.f, M2 = 0.f;
    {
        f32x2 st[8];
#pragma unroll
        for (int ww = 0; ww < 8; ++ww) { st[ww] = *(LDSP const f32x2*)(L + K2_RED + (ww * 8 + (c16 & 7)) * 8); mean += st[ww][0]; }
        mean *= 0.125f;
#pragma unroll
        for (int ww = 0; ww < 8; ++ww) { const float d = st[ww][0] - mean; M2 += st[ww][1] + 64.f * d * d; }
    }
    const float rstd = 1.f / sqrtf(M2 * (1.f / D) + LN_EPS);
    if (c16 < 8) {
#pragma unroll
        for (int t = 0; t < 4; ++t) {
            u32x2 o;
            o[0] = pack_bf16((hf[4 * t] - mean) * rstd, (hf[4 * t + 1] - mean) * rstd);
            o[1] = pack_bf16((hf[4 * t + 2] - mean) * rstd, (hf[4 * t + 3] - mean) * rstd);
            *(u32x2*)(HN + (((size_t)(row >> 5) * 32 + 4 * w + t) * 64 + (g4 >> 1) * 32 + (row & 31)) * 8 + 4 * (g4 & 1)) = o;
        }
#pragma unroll
        for (int t = 0; t < 4; ++t) *(f32x4*)(hbuf + (size_t)row * D + 64 * w + 16 * t + 4 * g4) = f32x4{hf[4 * t], hf[4 * t + 1], hf[4 * t + 2], hf[4 * t + 3]};
    }
    TS(1, 7);
}

constexpr int K4_LDS = 163840;
DI float gelu_tanh(float g) {
    constexpr float c0 = -2.f * 0.7978845608028654f * 1.4426950408889634f, c1 = c0 * 0.044715f;
    const float e = __builtin_amdgcn_exp2f(g * __builtin_fmaf(g * g, c1, c0));
    return g * __builtin_amdgcn_rcpf(1.f + e);
}
__global__ __launch_bounds__(256, 2) void k4_gateup(const u16* __restrict__ HN, const u16* __restrict__ WguT, u16* __restrict__ HH) {
    extern __shared__ __attribute__((aligned(16))) char smem[];
    const unsigned lds0 = (unsigned)(uintptr_t)smem;
    lds_ptr L = (lds_ptr)smem;
    const int tid = threadIdx.x, lane = tid & 63, w = (int)rfl(tid >> 6);
    const int bid = blockIdx.x, xcd = bid & 7, idx = bid >> 3;
    const int ib = (xcd >> 2) * 8 + (idx >> 2), jb = (xcd & 3) * 4 + (idx & 3);
    const int tb = 4 * ib + w;
    TS(2, 0);
    const u16* wsrc = WguT + (size_t)(7 * jb) * 32 * 512 + (size_t)w * 8 * 512 + lane * 8;
    auto issue_tile = [&](int t, int slot) {
        glds16x4(wsrc + (size_t)t * 32 * 512, lds0 + slot * 32768 + w * 8192);
        glds16x4(wsrc + ((size_t)t * 32 + 4) * 512, lds0 + slot * 32768 + w * 8192 + 4096);
    };
    bf16x8 xf[32];
    const u16* hp = HN + (size_t)tb * 32 * 512 + lane * 8;
    const u16* w0 = WguT + (size_t)(7 * jb) * 32 * 512 + (size_t)(2 * w) * 512 + lane * 8;
#define K4_QUARTER(q) do { \
        glds16(w0 + (size_t)(q) * 8 * 512, lds0 + (8 * (q) + 2 * w) * 1024); \
        glds16(w0 + (size_t)(q) * 8 * 512 + 512, lds0 + (8 * (q) + 2 * w + 1) * 1024); \
        _Pragma("unroll") for (int i = 2 * (q); i < 2 * (q) + 2; ++i) { const u16* p = hp + i * 2048; \
            GLD16(xf[4 * i], p, 0); GLD16(xf[4 * i + 1], p, 1024); GLD16(xf[4 * i + 2], p, 2048); GLD16(xf[4 * i + 3], p, 3072); } } while (0)
    K4_QUARTER(0); K4_QUARTER(1);
    f32x16 accp = {};
#pragma unroll
    for (int t = 0; t < 8; ++t) {
        const int slot = t % 5;
        lds_cptr wb = L + slot * 32768 + lane * 16;
        f32x16 acc = {};
        bf16x8 fa[2][4];
        float v[8];
        if (t == 0) {
            WAIT_VM_BAR(10);
#pragma unroll
            for (int i = 0; i < 8; ++i) PIN(xf[i]);
            TS(2, 1);
        }
        if (t < 7) {
#pragma unroll
            for (int i = 0; i < 4; ++i) fa[0][i] = ldsfrag(wb + i * 1024);
        }
#pragma unroll
        for (int b = 0; b < 8; ++b) {
            if (t == 0) {
                if (b == 0) K4_QUARTER(2);
                if (b == 1) { WAIT_VM_BAR(10); K4_QUARTER(3); }
                if (b == 2) issue_tile(1, 1);
                if (b == 3) { WAIT_VM_BAR(18); issue_tile(2, 2); }
                if (b == 5) { WAIT_VM_BAR(16); issue_tile(3, 3); }
                if (b == 6) issue_tile(4, 4);
                if ((b & 1) && b < 7) {
#pragma unroll
                    for (int i = 0; i < 8; ++i) PIN(xf[4 * (b + 1) + i]);
                }
            }
            if (t < 7) {
                if (b < 7) {
#pragma unroll
                    for (int i = 0; i < 4; ++i) fa[(b + 1) & 1][i] = ldsfrag(wb + (4 * (b + 1) + i) * 1024);
                }
                SBAR();
#pragma unroll
                for (int i = 0; i < 4; ++i) acc = mfma32(fa[b & 1][i], xf[4 * b + i], acc);
                PIN(acc);
            }
            if (t > 0) { v[b] = gelu_tanh(accp[b]) * accp[b + 8]; PIN(v[b]); }
            SBAR();
        }
        if (t > 0) {
            u32x4 o;
            o[0] = pack_bf16(v[0], v[1]); o[1] = pack_bf16(v[2], v[3]); o[2] = pack_bf16(v[4], v[5]); o[3] = pack_bf16(v[6], v[7]);
            *(u32x4*)(HH + ((size_t)tb * 112 + 7 * jb + t - 1) * 512 + lane * 8) = o;
        }
        accp = acc;
        if (t < 3) TS(2, 2 + 2 * t);
        if (t == 0) { WAIT_VM_BAR(24); issue_tile(5, 0); }
        else if (t == 1) { WAIT_VM_BAR(25); issue_tile(6, 1); }
        else if (t == 2) { WAIT_VM_BAR(26); }
        else if (t == 3) { WAIT_VM_BAR(19); }
        else if (t == 4) { WAIT_VM_BAR(12); }
        else if (t == 5) { WAIT_VM_BAR(4); }
        if (t < 3) TS(2, 3 + 2 * t);
    }
#undef K4_QUARTER
}

constexpr int K5_LDS = 65536;
__global__ __launch_bounds__(256, 1) void k5_down(const u16* __restrict__ HH, const u16* __restrict__ WdT, const float* __restrict__ hbuf, u16* __restrict__ out) {
    extern __shared__ __attribute__((aligned(16))) char smem[];
    lds_ptr L = (lds_ptr)smem;
    const int tid = threadIdx.x, lane = tid & 63, w = (int)rfl(tid >> 6);
    const int bid = blockIdx.x, xcd = bid & 7, idx = bid >> 3, ib = xcd * 4 + (idx >> 3), j = idx & 7;
    const u16* a0 = WdT + ((size_t)(2 * j) * 112 + 28 * w) * 512 + lane * 8;
    const u16* a1 = a0 + (size_t)112 * 512;
    const u16* b0 = HH + ((size_t)(2 * ib) * 112 + 28 * w) * 512 + lane * 8;
    const u16* b1 = b0 + (size_t)112 * 512;
    constexpr int P = K5P;
    TS(3, 0);
    bf16x8 fr[P][4];
    f32x16 acc[2][2] = {};
#pragma unroll
    for (int s = 0; s < P; ++s) {
        fr[s][0] = *(const bf16x8*)(a0 + s * 512); fr[s][1] = *(const bf16x8*)(a1 + s * 512);
        fr[s][2] = *(const bf16x8*)(b0 + s * 512); fr[s][3] = *(const bf16x8*)(b1 + s * 512);
        SBAR();
    }
#pragma unroll
    for (int s = 0; s < 28; ++s) {
        const int sl = s % P;
        acc[0][0] = mfma32(fr[sl][0], fr[sl][2], acc[0][0]);
        acc[0][1] = mfma32(fr[sl][0], fr[sl][3], acc[0][1]);
        acc[1][0] = mfma32(fr[sl][1], fr[sl][2], acc[1][0]);
        acc[1][1] = mfma32(fr[sl][1], fr[sl][3], acc[1][1]);
        SBAR();
        if (s + P < 28) {
            fr[sl][0] = *(const bf16x8*)(a0 + (s + P) * 512); fr[sl][1] = *(const bf16x8*)(a1 + (s + P) * 512);
            fr[sl][2] = *(const bf16x8*)(b0 + (s + P) * 512); fr[sl][3] = *(const bf16x8*)(b1 + (s + P) * 512);
        }
        SBAR();
    }
    TS(3, 1);
    const int r = lane & 31, h = lane >> 5;
#pragma unroll
    for (int q = 0; q < 4; ++q) {
        if (q != w) {
            const int slot = (w - q + 3) & 3;
#pragma unroll
            for (int g = 0; g < 4; ++g) {
                f32x4 v = {acc[q >> 1][q & 1][4 * g], acc[q >> 1][q & 1][4 * g + 1], acc[q >> 1][q & 1][4 * g + 2], acc[q >> 1][q & 1][4 * g + 3]};
                *(LDSP f32x4*)(L + ((q * 3 + slot) * 4 + g) * 1024 + lane * 16) = v;
            }
        }
    }
    const int na = w >> 1, tbq = w & 1;
    const size_t base = (size_t)(ib * 64 + 32 * tbq + r) * D + 64 * j + 32 * na + 4 * h;
    f32x4 xv[4];
#pragma unroll
    for (int g = 0; g < 4; ++g) xv[g] = *(const f32x4*)(hbuf + base + 8 * g);
    __syncthreads();
    TS(3, 2);
    f32x16 t = (w == 0) ? acc[0][0] : (w == 1) ? acc[0][1] : (w == 2) ? acc[1][0] : acc[1][1];
#pragma unroll
    for (int slot = 0; slot < 3; ++slot)
#pragma unroll
        for (int g = 0; g < 4; ++g) {
            const f32x4 v = *(LDSP const f32x4*)(L + ((w * 3 + slot) * 4 + g) * 1024 + lane * 16);
            t[4 * g] += v[0]; t[4 * g + 1] += v[1]; t[4 * g + 2] += v[2]; t[4 * g + 3] += v[3];
        }
#pragma unroll
    for (int g = 0; g < 4; ++g) {
        u32x2 ov;
        ov[0] = pack_bf16(t[4 * g] + xv[g][0], t[4 * g + 1] + xv[g][1]);
        ov[1] = pack_bf16(t[4 * g + 2] + xv[g][2], t[4 * g + 3] + xv[g][3]);
        *(u32x2*)(out + base + 8 * g) = ov;
    }
    TS(3, 3);
}

extern "C" void kernel_launch(void* const* d_in, const int* in_sizes, int n_in, void* d_out, int out_size, void* d_ws, size_t ws_size,
                              hipStream_t stream) {
    const u16* x = (const u16*)d_in[0];
    const u16* wq = (const u16*)d_in[1];
    const u16* wk = (const u16*)d_in[2];
    const u16* wv = (const u16*)d_in[3];
    const u16* wo = (const u16*)d_in[4];
    const u16* wg = (const u16*)d_in[6];
    const u16* wu = (const u16*)d_in[7];
    const u16* wd = (const u16*)d_in[8];
    char* ws = (char*)d_ws;
    u16* Qf = (u16*)(ws);
    u16* Kf = (u16*)(ws + (2 << 20));
    u16* Vf = (u16*)(ws + (2 << 20) + 65536);
    float* kmaxp = (float*)(ws + (2 << 20) + 131072);
    u16* WoT = (u16*)(ws + (3 << 20));
    u16* HN = (u16*)(ws + (4 << 20));
    float* hbuf = (float*)(ws + (24 << 20));
    u16* HH = (u16*)(ws + (8 << 20));
    u16* WguT = (u16*)(ws + (16 << 20));
    u16* WdT = (u16*)(ws + (20 << 20));
    (void)hipFuncSetAttribute((const void*)k1_ln_qkv, hipFuncAttributeMaxDynamicSharedMemorySize, K1_LDS);
    (void)hipFuncSetAttribute((const void*)k2_attn, hipFuncAttributeMaxDynamicSharedMemorySize, K2_LDS);
    (void)hipFuncSetAttribute((const void*)k4_gateup, hipFuncAttributeMaxDynamicSharedMemorySize, K4_LDS);
    (void)hipFuncSetAttribute((const void*)k5_down, hipFuncAttributeMaxDynamicSharedMemorySize, K5_LDS);
    k1_ln_qkv<<<256, 512, K1_LDS, stream>>>(x, wq, wk, wv, wo, wd, Qf, Kf, Vf, kmaxp, WoT, WdT);
    k2_attn<<<256, 512, K2_LDS, stream>>>(Qf, Kf, kmaxp, WoT, x, wg, wu, hbuf, HN, WguT);
    k4_gateup<<<256, 256, K4_LDS, stream>>>(HN, WguT, HH);
    k5_down<<<256, 256, K5_LDS, stream>>>(HH, WdT, hbuf, (u16*)d_out);
}
```

```cpp
#include <hip/hip_runtime.h>
#include <hip/hip_bf16.h>
#include <stdint.h>
#include <type_traits>

typedef unsigned short u16;
typedef __attribute__((ext_vector_type(8))) short bf16x8;
typedef __attribute__((ext_vector_type(4))) short s16x4;
typedef __attribute__((ext_vector_type(16))) float f32x16;
typedef __attribute__((ext_vector_type(4))) float f32x4;
typedef __attribute__((ext_vector_type(2))) float f32x2;
typedef __attribute__((ext_vector_type(4))) unsigned u32x4;
typedef __attribute__((ext_vector_type(2))) unsigned u32x2;
typedef __attribute__((ext_vector_type(2))) __bf16 bf16x2_t;

#define DI __device__ __forceinline__
#define LDSP __attribute__((address_space(3)))
typedef LDSP const char* lds_cptr;
typedef LDSP char* lds_ptr;

constexpr int S = 2048, D = 512, DF = 1792;
constexpr float LN_EPS = 1e-5f;
constexpr float QSCALE = 0.25f * 1.4426950408889634f;

DI unsigned pack_bf16(float a, float b) { f32x2 v = {a, b}; bf16x2_t r = __builtin_convertvector(v, bf16x2_t); return __builtin_bit_cast(unsigned, r); }
DI float bf_lo(unsigned u) { return __uint_as_float(u << 16); }
DI float bf_hi(unsigned u) { return __uint_as_float(u & 0xffff0000u); }
DI f32x16 mfma32(bf16x8 a, bf16x8 b, f32x16 c) { return __builtin_amdgcn_mfma_f32_32x32x16_bf16(a, b, c, 0, 0, 0); }
DI f32x4 mfma16(bf16x8 a, bf16x8 b, f32x4 c) { return __builtin_amdgcn_mfma_f32_16x16x32_bf16(a, b, c, 0, 0, 0); }
DI void glds16(const void* g, unsigned lds_base) {
    unsigned sv;
    asm volatile("s_mov_b32 %0, m0\n\ts_mov_b32 m0, %2\n\ts_nop 0\n\tglobal_load_lds_dwordx4 %1, off\n\ts_mov_b32 m0, %0"
                 : "=&s"(sv) : "v"(g), "s"(lds_base) : "memory");
}
DI void glds16x4(const void* g, unsigned lds_base) {
    unsigned sv;
    asm volatile("s_mov_b32 %0, m0\n\ts_mov_b32 m0, %2\n\ts_nop 0\n\tglobal_load_lds_dwordx4 %1, off\n\tglobal_load_lds_dwordx4 %1, off offset:1024\n\t"
                 "global_load_lds_dwordx4 %1, off offset:2048\n\tglobal_load_lds_dwordx4 %1, off offset:3072\n\ts_mov_b32 m0, %0"
                 : "=&s"(sv) : "v"(g), "s"(lds_base) : "memory");
}
template <int N> DI void glds_run(const u16* g, unsigned lds_base) {
#pragma unroll
    for (int i = 0; i + 4 <= N; i += 4) glds16x4(g + i * 512, lds_base + i * 1024);
#pragma unroll
    for (int i = N & ~3; i < N; ++i) glds16(g + i * 512, lds_base + i * 1024);
}
DI unsigned rfl(unsigned v) { return (unsigned)__builtin_amdgcn_readfirstlane((int)v); }
DI s16x4 trread(lds_cptr p) { return __builtin_amdgcn_ds_read_tr16_b64_v4i16((LDSP s16x4*)p); }
DI bf16x8 trfrag(lds_cptr p0, lds_cptr p1) { s16x4 a = trread(p0), b = trread(p1); return __builtin_shufflevector(a, b, 0, 1, 2, 3, 4, 5, 6, 7); }
DI bf16x8 ldsfrag(lds_cptr p) { return *(LDSP const bf16x8*)p; }
#define SBAR() __builtin_amdgcn_sched_barrier(0)
#define PIN(x) asm volatile("" : "+v"(x))
#define EX(x) __builtin_amdgcn_exp2f(x)
#define GLD16(dst, ptr, imm) asm volatile("global_load_dwordx4 %0, %1, off offset:%2" : "=v"(dst) : "v"(ptr), "i"(imm))
#define GST16_SC1(ptr, val) asm volatile("global_store_dwordx4 %0, %1, off sc1\n\ts_nop 1" :: "v"(ptr), "v"(val) : "memory")
#define GLD8(dst, ptr, imm) asm volatile("global_load_dwordx2 %0, %1, off offset:%2" : "=v"(dst) : "v"(ptr), "i"(imm))
#define K5P 14
#define ABL2 0
#define ABL4 0
#define TS(k, i)
#define WAIT_VM(N) asm volatile("s_waitcnt vmcnt(" #N ")" ::: "memory")
#define WAIT_VM_BAR(N) asm volatile("s_waitcnt vmcnt(" #N ") lgkmcnt(0)\n\ts_barrier" ::: "memory")

DI void ln_rows8(const u32x4 (&v)[8], int lane, lds_ptr tile, int row0) {
    const int rr = lane >> 3, cc = lane & 7;
    f32x2 f[32];
#pragma unroll
    for (int c = 0; c < 8; ++c)
#pragma unroll
        for (int e = 0; e < 4; ++e) f[4 * c + e] = f32x2{bf_lo(v[c][e]), bf_hi(v[c][e])};
    f32x2 s2 = f[0];
#pragma unroll
    for (int i = 1; i < 32; ++i) s2 += f[i];
    float s = s2[0] + s2[1];
    s += __shfl_xor(s, 1); s += __shfl_xor(s, 2); s += __shfl_xor(s, 4);
    const float mean = s * (1.f / D);
    const f32x2 m2 = {mean, mean};
    f32x2 q2 = {0.f, 0.f};
#pragma unroll
    for (int i = 0; i < 32; ++i) { f[i] -= m2; q2 += f[i] * f[i]; }
    float q = q2[0] + q2[1];
    q += __shfl_xor(q, 1); q += __shfl_xor(q, 2); q += __shfl_xor(q, 4);
    const float rstd = 1.f / sqrtf(q * (1.f / D) + LN_EPS);
    const f32x2 r2 = {rstd, rstd};
    const int row = row0 + rr;
#pragma unroll
    for (int c = 0; c < 8; ++c) {
        u32x4 o;
#pragma unroll
        for (int e = 0; e < 4; ++e) { const f32x2 t = f[4 * c + e] * r2; o[e] = pack_bf16(t[0], t[1]); }
        *(LDSP u32x4*)(tile + row * 1024 + (((cc + 8 * c) ^ (row & 15)) << 4)) = o;
    }
}

constexpr int K1_XN = 0, K1_WQ = 65536, K1_WKV = 131072, K1_ST = 147456, K1_LDS = K1_ST + 9216;
__global__ __launch_bounds__(512, 2) void k1_ln_qkv(const u16* __restrict__ x, const u16* __restrict__ wq, const u16* __restrict__ wk,
                                                  const u16* __restrict__ wv, const u16* __restrict__ wo, const u16* __restrict__ wd,
                                                  u16* __restrict__ Qf, u16* __restrict__ Kf, u16* __restrict__ Vf, float* __restrict__ kmaxp,
                                                  u16* __restrict__ WoT, u16* __restrict__ WdT, unsigned* __restrict__ done) {
    extern __shared__ __attribute__((aligned(16))) char smem[];
    const unsigned lds0 = (unsigned)(uintptr_t)smem;
    lds_ptr L = (lds_ptr)smem;
    const int tid = threadIdx.x, lane = tid & 63, w = (int)rfl(tid >> 6);
    const int bid = blockIdx.x, xcd = bid & 7, idx = bid >> 3, ib = xcd * 4 + (idx >> 3), j = idx & 7;
    const int row0 = ib * 64;
    TS(0, 0);
    if (tid == 0) done[bid] = 0u;
    u32x4 xv[8];
    {
        const u16* p = x + (size_t)(row0 + 8 * w + (lane >> 3)) * D + 8 * (lane & 7);
        GLD16(xv[0], p, 0); GLD16(xv[1], p, 128); GLD16(xv[2], p, 256); GLD16(xv[3], p, 384);
        GLD16(xv[4], p, 512); GLD16(xv[5], p, 640); GLD16(xv[6], p, 768); GLD16(xv[7], p, 896);
    }
    {
        const u16* src = wq + (size_t)((lane >> 2) & 7) * D + 64 * j + 32 * (lane >> 5) + 8 * (lane & 3);
#pragma unroll
        for (int n = 0; n < 8; ++n) { const int nn = w * 8 + n; glds16(src + (size_t)nn * 8 * D, lds0 + K1_WQ + nn * 1024); }
        const u16* s2 = ((j & 1) ? wv : wk) + lane * 8;
#pragma unroll
        for (int n = 0; n < 2; ++n) { const int nn = w * 2 + n; glds16(s2 + nn * 512, lds0 + K1_WKV + nn * 1024); }
        const int nt = bid & 15, sg = bid >> 4;
        {
            const u16* p = w < 7 ? wd + (size_t)(112 * sg + 16 * w + (lane >> 2)) * D + 32 * nt + 8 * (lane & 3)
                                 : wo + (size_t)(32 * sg + (lane >> 2)) * D + 32 * nt + 8 * (lane & 3);
            glds16(p, lds0 + K1_ST + w * 1024);
            if (w == 7) glds16(p + (size_t)16 * D, lds0 + K1_ST + 8 * 1024);
        }
    }
    TS(0, 1);
    WAIT_VM(11);
#pragma unroll
    for (int c = 0; c < 8; ++c) PIN(xv[c]);
    ln_rows8(xv, lane, L + K1_XN, 8 * w);
    TS(0, 2);
    WAIT_VM_BAR(0);
    TS(0, 3);
    const int r = lane & 31, h = lane >> 5, blk = (lane >> 4) & 1, q4 = (lane & 15) >> 2, p4 = lane & 3;
    const int wm = w & 1, wn = (w >> 1) & 1, kh = w >> 2;
    f32x16 acc = {};
    f32x4 akv = {};
    const int trow = 32 * wm + r;
    {
        lds_cptr wb = L + K1_WQ + kh * 32768 + h * 1024 + wn * 512 + q4 * 64 + 32 * blk + 8 * p4;
        lds_cptr xb = L + K1_XN + trow * 1024 + kh * 512;
        bf16x8 fa[16], fb[16];
#pragma unroll
        for (int i = 0; i < 16; ++i) { fa[i] = trfrag(wb + i * 2048, wb + i * 2048 + 256); fb[i] = ldsfrag(xb + ((((2 * i + h) & 15) ^ (trow & 15)) << 4) + (i >> 3) * 256); }
        const int g4 = lane >> 4, c16 = lane & 15, krow = 16 * (j >> 1) + c16;
        bf16x8 wf[2], xf[2];
#pragma unroll
        for (int ss = 0; ss < 2; ++ss) {
            const int s = 2 * w + ss;
            lds_cptr wp = L + K1_WKV + (32 * s + 8 * g4 + q4) * 32 + 8 * p4;
            wf[ss] = trfrag(wp, wp + 128);
            xf[ss] = ldsfrag(L + K1_XN + krow * 1024 + (((4 * s + g4) ^ (krow & 15)) << 4));
        }
#pragma unroll
        for (int i = 0; i < 16; ++i) acc = mfma32(fa[i], fb[i], acc);
#pragma unroll
        for (int ss = 0; ss < 2; ++ss) akv = (j & 1) ? mfma16(xf[ss], wf[ss], akv) : mfma16(wf[ss], xf[ss], akv);
    }
    TS(0, 4);
    {
        const int nt = bid & 15, sg = bid >> 4;
        if (w < 7) {
            lds_cptr p = L + K1_ST + w * 1024 + (4 * h + q4) * 64 + 32 * blk + 8 * p4;
            bf16x8 f = trfrag(p, p + 512);
            *(bf16x8*)(WdT + ((size_t)nt * 112 + 7 * sg + w) * 512 + lane * 8) = f;
        } else {
            const int g4 = lane >> 4;
#pragma unroll
            for (int n = 0; n < 2; ++n) {
                lds_cptr p = L + K1_ST + 7 * 1024 + (8 * g4 + q4) * 64 + 32 * n + 8 * p4;
                bf16x8 f = trfrag(p, p + 256);
                *(bf16x8*)(WoT + ((size_t)(2 * nt + n) * 16 + sg) * 512 + lane * 8) = f;
            }
        }
    }
    asm volatile("s_waitcnt lgkmcnt(0)\n\ts_barrier" ::: "memory");
    {
        LDSP f32x4* ex = (LDSP f32x4*)(L + K1_WQ + w * 2048 + lane * 16);
        const int o = kh ? 0 : 8;
        f32x4 e0, e1;
#pragma unroll
        for (int e = 0; e < 4; ++e) { e0[e] = kh ? acc[e] : acc[8 + e]; e1[e] = kh ? acc[4 + e] : acc[12 + e]; }
        (void)o;
        ex[0] = e0; ex[64] = e1;
        *(LDSP f32x4*)(L + K1_WQ + 16384 + w * 1024 + lane * 16) = akv;
    }
    __syncthreads();
    {
        LDSP const f32x4* ex = (LDSP const f32x4*)(L + K1_WQ + (w ^ 4) * 2048 + lane * 16);
        const f32x4 e0 = ex[0], e1 = ex[64];
        const int grow = row0 + trow;
#pragma unroll
        for (int gg = 0; gg < 2; ++gg) {
            const int g = 2 * kh + gg;
            float v[4];
#pragma unroll
            for (int e = 0; e < 4; ++e) v[e] = ((kh ? acc[8 + 4 * gg + e] : acc[4 * gg + e]) + (gg ? e1[e] : e0[e])) * QSCALE;
            const int head = (64 * j + 32 * wn) / 16 + (g >> 1);
            u32x2 o; o[0] = pack_bf16(v[0], v[1]); o[1] = pack_bf16(v[2], v[3]);
            u16* dst = Qf + ((((size_t)(grow >> 3) * 8 + (head >> 2)) * 2 + (g & 1)) * 32 + (head & 3) * 8 + (grow & 7)) * 8 + 4 * h;
            *(u32x2*)dst = o;
        }
    }
    if (w == 0) {
        const int g4 = lane >> 4, c16 = lane & 15;
        f32x4 t = akv;
#pragma unroll
        for (int ww = 1; ww < 8; ++ww) { f32x4 u = *(LDSP const f32x4*)(L + K1_WQ + 16384 + ww * 1024 + lane * 16); t += u; }
        u32x2 o; o[0] = pack_bf16(t[0], t[1]); o[1] = pack_bf16(t[2], t[3]);
        const int tok0 = row0 + 16 * (j >> 1);
        if (j & 1) {
            const int kb = tok0 >> 5, s = (tok0 >> 4) & 1;
            *(u32x2*)(Vf + ((((size_t)kb * 2 + s) * 2 + (g4 & 1)) * 16 + c16) * 8 + 4 * (g4 >> 1)) = o;
        } else {
            const int key = tok0 + c16;
            *(u32x2*)(Kf + (((size_t)(key >> 5) * 2 + (g4 >> 1)) * 32 + (key & 31)) * 8 + 4 * (g4 & 1)) = o;
            float a = bf_lo(o[0]), b = bf_hi(o[0]), c = bf_lo(o[1]), d = bf_hi(o[1]);
            float n2 = a * a + b * b + c * c + d * d;
            n2 += __shfl_xor(n2, 16); n2 += __shfl_xor(n2, 32);
            n2 = fmaxf(n2, __shfl_xor(n2, 1)); n2 = fmaxf(n2, __shfl_xor(n2, 2)); n2 = fmaxf(n2, __shfl_xor(n2, 4)); n2 = fmaxf(n2, __shfl_xor(n2, 8));
            if (lane == 0) kmaxp[tok0 >> 4] = n2;
        }
    }
    TS(0, 5);
}

constexpr int K2_K = 0, K2_V = 65536, K2_C = 131072, K2_Q = 135168, K2_KM = 143488, K2_ST = 144000, K2_RED = 158336, K2_LDS = 158848;
constexpr int K2_OSTR = 1040;
struct AttnState { f32x16 sA, sB, O; bf16x8 kf, va0, va1, vb0, vb1; lds_cptr kp, vp; };
#define EXP4(SC, o) do { _Pragma("unroll") for (int i = (o); i < (o) + 4; ++i) { SC[i] = EX(SC[i]); } PIN(SC); SBAR(); } while (0)
#define ATT_STEP(SC, SN, V0, V1, W0, W1, KOFF, VOFF) do { \
        u32x4 p0, p1; \
        EXP4(SC, 0); \
        SN = mfma32(kf, qf, cinit); PIN(SN); \
        kf = ldsfrag(kp + (KOFF)); W0 = ldsfrag(vp + (VOFF)); W1 = ldsfrag(vp + (VOFF) + 512); \
        SBAR(); \
        EXP4(SC, 4); \
        EXP4(SC, 8); \
        _Pragma("unroll") for (int i = 0; i < 4; ++i) p0[i] = pack_bf16(SC[2 * i], SC[2 * i + 1]); \
        PIN(p0); SBAR(); \
        O = mfma32(V0, __builtin_bit_cast(bf16x8, p0), O); PIN(O); \
        SBAR(); \
        EXP4(SC, 12); \
        _Pragma("unroll") for (int i = 0; i < 4; ++i) p1[i] = pack_bf16(SC[8 + 2 * i], SC[8 + 2 * i + 1]); \
        PIN(p1); SBAR(); \
        O = mfma32(V1, __builtin_bit_cast(bf16x8, p1), O); PIN(O); \
        SBAR(); \
    } while (0)
#define ATT_RUN(N4) for (int it = 0; it < (N4); ++it) { \
        ATT_STEP(sA, sB, va0, va1, vb0, vb1, 2048, 1024); \
        ATT_STEP(sB, sA, vb0, vb1, va0, va1, 3072, 2048); \
        ATT_STEP(sA, sB, va0, va1, vb0, vb1, 4096, 3072); \
        vp += vinc; \
        ATT_STEP(sB, sA, vb0, vb1, va0, va1, 5120, 0); \
        kp += 4096; }
#define ATT_INIT() do { kp = L + K2_K + lane * 16; vp = vp0; O = f32x16{}; \
        kf = ldsfrag(kp); va0 = ldsfrag(vp); va1 = ldsfrag(vp + 512); sA = mfma32(kf, qf, cinit); kf = ldsfrag(kp + 1024); SBAR(); } while (0)
__global__ __launch_bounds__(512, 2) void k2_attn(const u16* __restrict__ Qf, const u16* __restrict__ KVf,
                                               const float* __restrict__ kmaxp, const u16* __restrict__ WoT, const u16* __restrict__ x,
                                               const u16* __restrict__ wg, const u16* __restrict__ wu,
                                               float* __restrict__ hbuf, u16* __restrict__ HN, u16* __restrict__ WguT) {
    extern __shared__ __attribute__((aligned(16))) char smem[];
    const unsigned lds0 = (unsigned)(uintptr_t)smem;
    lds_ptr L = (lds_ptr)smem;
    const int tid = threadIdx.x, lane = tid & 63, w = (int)rfl(tid >> 6);
    const int rg = blockIdx.x;
    const int r = lane & 31, h = lane >> 5;
    TS(1, 0);
    glds16(Qf + (size_t)rg * 4096 + w * 512 + lane * 8, lds0 + K2_Q + w * 1024);
    if (lane < 32) glds16(kmaxp + lane * 4, lds0 + K2_KM);
    const int kv = (w >> 2) * 64, wq4 = w & 3;
    glds_run<5>(KVf + (size_t)(kv + 5 * wq4) * 512 + lane * 8, lds0 + K2_K + (kv + 5 * wq4) * 1024);
    const u16* kvrest = KVf + (size_t)(kv + 20 + 11 * wq4) * 512 + lane * 8;
    const unsigned kvdst = lds0 + K2_K + (kv + 20 + 11 * wq4) * 1024;
    const int sp = rg & 15, jb = rg >> 4;
    if (tid < 256) {
        const int b = tid >> 5, sl = tid & 31;
        const unsigned val = sl == 0 ? 0x3f803f80u : 0u;
        *(LDSP u32x4*)(L + K2_C + b * 512 + sl * 16) = u32x4{val, val, val, val};
    }
    WAIT_VM_BAR(0);
    TS(1, 1);
    glds_run<4>(kvrest, kvdst);
    const bf16x8 qf = ldsfrag(L + K2_Q + w * 1024 + lane * 16);
    float m;
    {
        float km = fmaxf(*(LDSP const float*)(L + K2_KM + lane * 4), *(LDSP const float*)(L + K2_KM + 256 + lane * 4));
#pragma unroll
        for (int mm = 1; mm < 64; mm <<= 1) km = fmaxf(km, __shfl_xor(km, mm));
        float q2 = 0.f;
        const u32x4 qu = __builtin_bit_cast(u32x4, qf);
#pragma unroll
        for (int e = 0; e < 4; ++e) { float a = bf_lo(qu[e]), b = bf_hi(qu[e]); q2 += a * a + b * b; }
        q2 += __shfl_xor(q2, 32);
        m = sqrtf(q2 * km) * 1.001f + 1e-6f;
    }
    f32x16 cinit;
#pragma unroll
    for (int i = 0; i < 16; ++i) cinit[i] = -m;
    const bool isv = r < 16;
    const int cslot = (r == 16) ? 0 : ((r >= 20 && r < 28) ? 64 : 16);
    const lds_cptr vp0 = isv ? (L + K2_V + h * 256 + r * 16) : (L + K2_C + cslot);
    const int vinc = isv ? 4096 : 0;
    f32x16 sA, sB, O; bf16x8 kf, va0, va1, vb0, vb1; lds_cptr kp, vp;
    ATT_INIT();
    ATT_RUN(1);
    glds_run<4>(kvrest + 4 * 512, kvdst + 4096);
    ATT_RUN(1);
    glds_run<3>(kvrest + 8 * 512, kvdst + 8192);
    ATT_RUN(1);
    if (w < 7) {
#pragma unroll
        for (int n = 0; n < 2; ++n) {
            const int c = w * 64 + lane, row = c / 14, ch = c - row * 14;
            glds16((n ? wu : wg) + (size_t)(32 * sp + row) * DF + 112 * jb + 8 * ch, lds0 + K2_ST + n * 7168 + w * 1024);
        }
    }
    ATT_RUN(1);
    TS(1, 2);
    WAIT_VM_BAR(0);
    const int blk = (lane >> 4) & 1, q4 = (lane & 15) >> 2, p4 = lane & 3;
    if (w < 7) {
#pragma unroll
        for (int s2 = 0; s2 < 2; ++s2) {
            lds_cptr p = L + K2_ST + blk * 7168 + (16 * s2 + 8 * h + q4) * 224 + 32 * w + 8 * p4;
            bf16x8 f = trfrag(p, p + 4 * 224);
            *(bf16x8*)(WguT + ((size_t)(7 * jb + w) * 32 + 2 * sp + s2) * 512 + lane * 8) = f;
        }
    }
    const int c16 = lane & 15, g4 = lane >> 4;
    bf16x8 wf[2][16];
    u32x2 xr[4];
    const u16* wp0 = WoT + (size_t)(4 * w) * 16 * 512 + lane * 8;
#define WO_BURST(i0) do { if (ABL2 == 1) { _Pragma("unroll") for (int i = 0; i < 8; ++i) wf[(i0) >> 2][(4 * (i0) + i) & 15] = qf; break; } _Pragma("unroll") for (int i = (i0); i < (i0) + 2; ++i) { const u16* p = wp0 + i * 2048; \
        GLD16(wf[i >> 2][4 * (i & 3)], p, 0); GLD16(wf[i >> 2][4 * (i & 3) + 1], p, 1024); GLD16(wf[i >> 2][4 * (i & 3) + 2], p, 2048); GLD16(wf[i >> 2][4 * (i & 3) + 3], p, 3072); } } while (0)
    WO_BURST(0);
    TS(1, 3);
    ATT_RUN(3);
    WO_BURST(2);
    ATT_RUN(3);
    asm volatile("s_barrier" ::: "memory");
    if (ABL2 != 1) {
    glds_run<5>(wp0 + (size_t)32 * 512, lds0 + K2_K + 5 * w * 1024);
    glds_run<5>(wp0 + (size_t)37 * 512, lds0 + K2_V + 5 * w * 1024);
    }
    WO_BURST(4);
    ATT_RUN(3);
    WO_BURST(6);
    {
        const u16* xp = x + (size_t)(rg * 8 + (c16 & 7)) * D + 64 * w + 4 * g4;
        GLD8(xr[0], xp, 0); GLD8(xr[1], xp, 32); GLD8(xr[2], xp, 64); GLD8(xr[3], xp, 96);
    }
    ATT_RUN(3);
    TS(1, 4);
    float l = __shfl(O[8], r);
    if (__any(!(l >= 5.9604645e-8f))) {
        asm volatile("s_waitcnt vmcnt(0)" ::: "memory");
        const u16* kg = KVf + lane * 8;
        const u16* vg = KVf + 64 * 512 + h * 128 + (r & 15) * 8;
        const unsigned cv = (r == 16) ? 0x3f803f80u : 0u;
        const u32x4 cst = {cv, cv, cv, cv};
        float mx = -INFINITY;
        const f32x16 z = {};
        for (int kb = 0; kb < 64; ++kb) {
            const f32x16 s = mfma32(*(const bf16x8*)(kg + kb * 512), qf, z);
#pragma unroll
            for (int i = 0; i < 16; ++i) mx = fmaxf(mx, s[i]);
        }
        mx = fmaxf(mx, __shfl_xor(mx, 32));
#pragma unroll
        for (int i = 0; i < 16; ++i) cinit[i] = -mx;
        O = f32x16{};
        for (int kb = 0; kb < 64; ++kb) {
            const f32x16 s = mfma32(*(const bf16x8*)(kg + kb * 512), qf, cinit);
            u32x4 v0 = *(const u32x4*)(vg + kb * 512), v1 = *(const u32x4*)(vg + kb * 512 + 256);
            if (!isv) { v0 = cst; v1 = cst; }
            u32x4 p0, p1;
#pragma unroll
            for (int i = 0; i < 4; ++i) { p0[i] = pack_bf16(EX(s[2 * i]), EX(s[2 * i + 1])); p1[i] = pack_bf16(EX(s[8 + 2 * i]), EX(s[8 + 2 * i + 1])); }
            O = mfma32(__builtin_bit_cast(bf16x8, v0), __builtin_bit_cast(bf16x8, p0), O);
            O = mfma32(__builtin_bit_cast(bf16x8, v1), __builtin_bit_cast(bf16x8, p1), O);
        }
        l = __shfl(O[8], r);
    }
    bf16x8 wl[22];
#pragma unroll
    for (int i = 0; i < 22; ++i) wl[i] = *(const bf16x8*)(wp0 + (size_t)(42 + i) * 512);
    {
        const float inv = 1.f / l;
        lds_ptr dst = L + K2_Q + (r & 7) * K2_OSTR + ((4 * w + (r >> 3)) * 16 + 4 * h) * 2;
        u32x2 a, b;
        a[0] = pack_bf16(O[0] * inv, O[1] * inv); a[1] = pack_bf16(O[2] * inv, O[3] * inv);
        b[0] = pack_bf16(O[4] * inv, O[5] * inv); b[1] = pack_bf16(O[6] * inv, O[7] * inv);
        *(LDSP u32x2*)dst = a; *(LDSP u32x2*)(dst + 16) = b;
    }
    asm volatile("s_waitcnt vmcnt(22) lgkmcnt(0)\n\ts_barrier" ::: "memory");
    TS(1, 5);
#pragma unroll
    for (int i = 0; i < 16; ++i) { PIN(wf[0][i]); PIN(wf[1][i]); }
#pragma unroll
    for (int i = 0; i < 4; ++i) PIN(xr[i]);
    f32x4 acc[4] = {};
    lds_cptr ob = L + K2_Q + (c16 & 7) * K2_OSTR + g4 * 16;
#pragma unroll
    for (int s = 0; s < 16; ++s) {
        const bf16x8 b = ldsfrag(ob + s * 64);
        acc[0] = mfma16(wf[0][s], b, acc[0]);
        acc[1] = mfma16(wf[1][s], b, acc[1]);
        const bf16x8 a2 = s < 10 ? ldsfrag(L + (s < 5 ? K2_K + (5 * w + s) * 1024 : K2_V + (5 * w + s - 5) * 1024) + lane * 16) : wl[s - 10];
        acc[2] = mfma16(a2, b, acc[2]);
        acc[3] = mfma16(wl[6 + s], b, acc[3]);
    }
    PIN(acc[3]);
    TS(1, 6);
    float hf[16];
#pragma unroll
    for (int t = 0; t < 4; ++t) {
        hf[4 * t] = acc[t][0] + bf_lo(xr[t][0]); hf[4 * t + 1] = acc[t][1] + bf_hi(xr[t][0]);
        hf[4 * t + 2] = acc[t][2] + bf_lo(xr[t][1]); hf[4 * t + 3] = acc[t][3] + bf_hi(xr[t][1]);
    }
    float s1 = 0.f;
#pragma unroll
    for (int i = 0; i < 16; ++i) s1 += hf[i];
    s1 += __shfl_xor(s1, 16); s1 += __shfl_xor(s1, 32);
    const float mw = s1 * (1.f / 64.f);
    float m2 = 0.f;
#pragma unroll
    for (int i = 0; i < 16; ++i) { const float d = hf[i] - mw; m2 += d * d; }
    m2 += __shfl_xor(m2, 16); m2 += __shfl_xor(m2, 32);
    if (lane < 8) *(LDSP f32x2*)(L + K2_RED + (w * 8 + lane) * 8) = f32x2{mw, m2};
    const int row = rg * 8 + (c16 & 7);
    asm volatile("s_waitcnt lgkmcnt(0)\n\ts_barrier" ::: "memory");
    float mean = 0.f, M2 = 0.f;
    {
        f32x2 st[8];
#pragma unroll
        for (int ww = 0; ww < 8; ++ww) { st[ww] = *(LDSP const f32x2*)(L + K2_RED + (ww * 8 + (c16 & 7)) * 8); mean += st[ww][0]; }
        mean *= 0.125f;
#pragma unroll
        for (int ww = 0; ww < 8; ++ww) { const float d = st[ww][0] - mean; M2 += st[ww][1] + 64.f * d * d; }
    }
    const float rstd = 1.f / sqrtf(M2 * (1.f / D) + LN_EPS);
    if (c16 < 8) {
#pragma unroll
        for (int t = 0; t < 4; ++t) {
            u32x2 o;
            o[0] = pack_bf16((hf[4 * t] - mean) * rstd, (hf[4 * t + 1] - mean) * rstd);
            o[1] = pack_bf16((hf[4 * t + 2] - mean) * rstd, (hf[4 * t + 3] - mean) * rstd);
            *(u32x2*)(HN + (((size_t)(row >> 5) * 32 + 4 * w + t) * 64 + (g4 >> 1) * 32 + (row & 31)) * 8 + 4 * (g4 & 1)) = o;
        }
#pragma unroll
        for (int t = 0; t < 4; ++t) *(f32x4*)(hbuf + (size_t)row * D + 64 * w + 16 * t + 4 * g4) = f32x4{hf[4 * t], hf[4 * t + 1], hf[4 * t + 2], hf[4 * t + 3]};
    }
    TS(1, 7);
}

constexpr int K4_LDS = 163840;
DI float gelu_tanh(float g) {
    constexpr float c0 = -2.f * 0.7978845608028654f * 1.4426950408889634f, c1 = c0 * 0.044715f;
    const float e = __builtin_amdgcn_exp2f(g * __builtin_fmaf(g * g, c1, c0));
    return g * __builtin_amdgcn_rcpf(1.f + e);
}
template <class Hook>
DI void k4_tile(int bid, const u16* __restrict__ HN, const u16* __restrict__ WguT, u16* __restrict__ HH, char* smem, Hook&& hook) {
    const unsigned lds0 = (unsigned)(uintptr_t)smem;
    lds_ptr L = (lds_ptr)smem;
    const int tid = threadIdx.x, lane = tid & 63, w = (int)rfl(tid >> 6);
    const int xcd = bid & 7, idx = bid >> 3;
    const int ib = (xcd >> 2) * 8 + (idx >> 2), jb = (xcd & 3) * 4 + (idx & 3);
    const int tb = 4 * ib + w;
    TS(2, 0);
    const u16* wsrc = WguT + (size_t)(7 * jb) * 32 * 512 + (size_t)w * 8 * 512 + lane * 8;
    auto issue_tile = [&](int t, int slot) {
        glds16x4(wsrc + (size_t)t * 32 * 512, lds0 + slot * 32768 + w * 8192);
        glds16x4(wsrc + ((size_t)t * 32 + 4) * 512, lds0 + slot * 32768 + w * 8192 + 4096);
    };
    bf16x8 xf[32];
    const u16* hp = HN + (size_t)tb * 32 * 512 + lane * 8;
    const u16* w0 = WguT + (size_t)(7 * jb) * 32 * 512 + (size_t)(2 * w) * 512 + lane * 8;
#define K4_QUARTER(q) do { \
        glds16(w0 + (size_t)(q) * 8 * 512, lds0 + (8 * (q) + 2 * w) * 1024); \
        glds16(w0 + (size_t)(q) * 8 * 512 + 512, lds0 + (8 * (q) + 2 * w + 1) * 1024); \
        _Pragma("unroll") for (int i = 2 * (q); i < 2 * (q) + 2; ++i) { const u16* p = hp + i * 2048; \
            GLD16(xf[4 * i], p, 0); GLD16(xf[4 * i + 1], p, 1024); GLD16(xf[4 * i + 2], p, 2048); GLD16(xf[4 * i + 3], p, 3072); } } while (0)
    K4_QUARTER(0); K4_QUARTER(1);
    f32x16 accp = {};
    bf16x8 fa[2][4];
    WAIT_VM_BAR(10);
#pragma unroll
    for (int i = 0; i < 8; ++i) PIN(xf[i]);
    TS(2, 1);
#pragma unroll
    for (int i = 0; i < 4; ++i) fa[0][i] = ldsfrag(L + lane * 16 + i * 1024);
#pragma unroll
    for (int t = 0; t < 8; ++t) {
        f32x16 acc = {};
        float v[8];
#pragma unroll
        for (int b = 0; b < 8; ++b) {
            if (t == 0) {
                if (b == 0) K4_QUARTER(2);
                if (b == 1) { WAIT_VM_BAR(10); K4_QUARTER(3); }
                if (b == 2) issue_tile(1, 1);
                if (b == 3) { WAIT_VM_BAR(18); issue_tile(2, 2); }
                if (b == 5) { WAIT_VM_BAR(16); issue_tile(3, 3); }
                if (b == 6) issue_tile(4, 4);
                if (b == 7) { WAIT_VM_BAR(24); }
                if ((b & 1) && b < 7) {
#pragma unroll
                    for (int i = 0; i < 8; ++i) PIN(xf[4 * (b + 1) + i]);
                }
            } else if (b == 3) {
                if (t == 1) { WAIT_VM_BAR(16); issue_tile(5, 0); }
                else if (t == 2) { WAIT_VM_BAR(17); issue_tile(6, 1); }
                else if (t == 3) { WAIT_VM_BAR(18); }
                else if (t == 4) { WAIT_VM_BAR(43); }
                else if (t == 5) { WAIT_VM_BAR(35); }
            }
            if (t == 3 && b >= 3) hook(std::integral_constant<int, 0>{}, b - 3);
            if (t == 4 && b < 3) hook(std::integral_constant<int, 0>{}, b + 5);
            if (t < 7) {
                if (b < 7 || t < 6) {
                    lds_cptr nb = L + (b < 7 ? (t % 5) * 32768 + 4 * (b + 1) * 1024 : ((t + 1) % 5) * 32768) + lane * 16;
#pragma unroll
                    for (int i = 0; i < 4; ++i) fa[(b + 1) & 1][i] = ldsfrag(nb + i * 1024);
                }
                SBAR();
#pragma unroll
                for (int i = 0; i < 4; ++i) acc = mfma32(fa[b & 1][i], xf[4 * b + i], acc);
            }
            if (t > 0) { v[b] = gelu_tanh(accp[b]) * accp[b + 8]; PIN(v[b]); }
            SBAR();
        }
        if (t > 0) {
            u32x4 o;
            o[0] = pack_bf16(v[0], v[1]); o[1] = pack_bf16(v[2], v[3]); o[2] = pack_bf16(v[4], v[5]); o[3] = pack_bf16(v[6], v[7]);
            GST16_SC1(HH + ((size_t)tb * 112 + 7 * jb + t - 1) * 512 + lane * 8, o);
        }
        accp = acc;
        if (t < 6) TS(2, 2 + t);
    }
#undef K4_QUARTER
}

template <int I, int N, class F> DI void static_for(F&& f) {
    if constexpr (I < N) { f(std::integral_constant<int, I>{}); static_for<I + 1, N>(f); }
}
template <int N> DI void wait_vm() { asm volatile("s_waitcnt vmcnt(%0)" :: "i"(N) : "memory"); }
constexpr int k5_wait(int s, int PV, int PH) {
    int issued = 2 * PV + 2 * PH, pw = s < PV ? 2 * s + 1 : -1, ph = s < PH ? 2 * PV + 2 * s + 1 : -1;
    for (int t = 0; t < s; ++t) {
        if (t + PH < 28) { if (t + PH == s) ph = issued + 1; issued += 2; }
    }
    return issued - 1 - (pw > ph ? pw : ph);
}
#define GLD16_A(dst, ptr, imm) asm volatile("global_load_dwordx4 %0, %1, off offset:%2" : "=a"(dst) : "v"(ptr), "i"(imm))
#define PIN_A(x) asm volatile("" : "+a"(x))
#define GLD16_SC1(dst, ptr, imm) asm volatile("global_load_dwordx4 %0, %1, off offset:%2 sc1" : "=v"(dst) : "v"(ptr), "i"(imm))
__global__ __launch_bounds__(256, 1) void k45_ffn(const u16* __restrict__ HN, const u16* __restrict__ WguT, u16* __restrict__ HH,
                                                  const u16* __restrict__ WdT, const float* __restrict__ hbuf, u16* __restrict__ out,
                                                  unsigned* __restrict__ done) {
    extern __shared__ __attribute__((aligned(16))) char smem[];
    lds_ptr L = (lds_ptr)smem;
    const int tid = threadIdx.x, lane = tid & 63, w = (int)rfl(tid >> 6);
    const int bid = blockIdx.x;
    const int xcd = bid & 7, idx = bid >> 3, ib = xcd * 4 + (idx >> 3), j = idx & 7;
    const u16* a0 = WdT + ((size_t)(2 * j) * 112 + 28 * w) * 512 + lane * 8;
    const u16* a1 = a0 + (size_t)112 * 512;
    const u16* b0 = HH + ((size_t)(2 * ib) * 112 + 28 * w) * 512 + lane * 8;
    const u16* b1 = b0 + (size_t)112 * 512;
    constexpr int PW = 28, PV = 12, PH = 10;
    bf16x8 wa[PW][2];
    int job = bid;
    for (;;) {
        k4_tile(job, HN, WguT, HH, smem, [&](auto, int part) {
            static_for<0, 8>([&](auto pc) {
                constexpr int pp = decltype(pc)::value;
                if (part == pp) {
                    static_for<PV + 2 * pp, PV + 2 * pp + 2>([&](auto ic) {
                        constexpr int s = decltype(ic)::value;
                        const u16* pa = a0 + (s & ~3) * 512; const u16* pb = a1 + (s & ~3) * 512;
                        bf16x8& d0 = wa[s][0]; bf16x8& d1 = wa[s][1];
                        GLD16_A(d0, pa, (s & 3) * 1024); GLD16_A(d1, pb, (s & 3) * 1024);
                    });
                }
            });
        });
        TS(3, 0);
        asm volatile("s_waitcnt vmcnt(0) lgkmcnt(0)\n\ts_barrier" ::: "memory");
        if (tid == 0) {
            const int jx = job & 7, ji = job >> 3;
            const unsigned one = 1u;
            asm volatile("global_store_dword %0, %1, off sc1" :: "v"(done + ((jx >> 2) * 8 + (ji >> 2)) * 16 + (jx & 3) * 4 + (ji & 3)), "v"(one) : "memory");
        }
        static_for<0, PV>([&](auto ic) {
            constexpr int s = decltype(ic)::value;
            const u16* pa = a0 + (s & ~3) * 512; const u16* pb = a1 + (s & ~3) * 512;
            bf16x8& d0 = wa[s][0]; bf16x8& d1 = wa[s][1];
            GLD16(d0, pa, (s & 3) * 1024); GLD16(d1, pb, (s & 3) * 1024);
        });
        LDSP int* miss = (LDSP int*)L;
        if (w == 0) {
            int m = -1;
            const unsigned* fl = done + (ib >> 1) * 16 + (lane & 15);
            for (int spin = 0; spin < 4096; ++spin) {
                unsigned f;
                asm volatile("global_load_dword %0, %1, off sc1\n\ts_waitcnt vmcnt(0)" : "=v"(f) : "v"(fl) : "memory");
                const unsigned long long bad = __ballot(f == 0u) & 0xffffull;
                m = bad ? (int)__builtin_ctzll(bad) : -1;
                if (m < 0) break;
                __builtin_amdgcn_s_sleep(2);
            }
            if (lane == 0) *miss = m;
        }
        asm volatile("s_waitcnt lgkmcnt(0)\n\ts_barrier" ::: "memory");
        const int m = (int)rfl((unsigned)*miss);
        if (m < 0) break;
        asm volatile("s_waitcnt vmcnt(0) lgkmcnt(0)\n\ts_barrier" ::: "memory");
        const int ibp = ib >> 1, jbp = m;
        job = ((ibp & 7) * 4 + (jbp & 3)) * 8 + ((ibp >> 3) * 4 + (jbp >> 2));
    }
    TS(3, 1);
    const int r = lane & 31, h = lane >> 5;
    const int na = w >> 1, tbq = w & 1;
    const size_t base = (size_t)(ib * 64 + 32 * tbq + r) * D + 64 * j + 32 * na + 4 * h;
    f32x4 xv[4];
    { const float* hp = hbuf + base; GLD16(xv[0], hp, 0); GLD16(xv[1], hp, 32); GLD16(xv[2], hp, 64); GLD16(xv[3], hp, 96); }
    bf16x8 hb[PH][2];
#pragma unroll
    for (int s = 0; s < PH; ++s) { GLD16_SC1(hb[s][0], b0 + s * 512, 0); GLD16_SC1(hb[s][1], b1 + s * 512, 0); }
    f32x16 acc[2][2] = {};
    static_for<0, 28>([&](auto ic) {
        constexpr int s = decltype(ic)::value, sw = s % PW, sh = s % PH;
        wait_vm<k5_wait(s, PV, PH)>();
        bf16x8& w0 = wa[sw][0]; bf16x8& w1 = wa[sw][1]; bf16x8& h0 = hb[sh][0]; bf16x8& h1 = hb[sh][1];
        if constexpr (s < PV) { PIN(w0); PIN(w1); } else { PIN_A(w0); PIN_A(w1); }
        PIN(h0); PIN(h1);
        acc[0][0] = mfma32(wa[sw][0], hb[sh][0], acc[0][0]);
        acc[0][1] = mfma32(wa[sw][0], hb[sh][1], acc[0][1]);
        acc[1][0] = mfma32(wa[sw][1], hb[sh][0], acc[1][0]);
        acc[1][1] = mfma32(wa[sw][1], hb[sh][1], acc[1][1]);
        SBAR();
        const u16* pb0 = b0 + (s + PH) * 512; const u16* pb1 = b1 + (s + PH) * 512;
        if constexpr (s + PH < 28) { GLD16_SC1(h0, pb0, 0); GLD16_SC1(h1, pb1, 0); }
        SBAR();
    });
    TS(3, 2);
#pragma unroll
    for (int q = 0; q < 4; ++q) {
        if (q != w) {
            const int slot = (w - q + 3) & 3;
#pragma unroll
            for (int g = 0; g < 4; ++g) {
                f32x4 v = {acc[q >> 1][q & 1][4 * g], acc[q >> 1][q & 1][4 * g + 1], acc[q >> 1][q & 1][4 * g + 2], acc[q >> 1][q & 1][4 * g + 3]};
                *(LDSP f32x4*)(L + ((q * 3 + slot) * 4 + g) * 1024 + lane * 16) = v;
            }
        }
    }
    asm volatile("s_waitcnt lgkmcnt(0)\n\ts_barrier" ::: "memory");
#pragma unroll
    for (int g = 0; g < 4; ++g) PIN(xv[g]);
    f32x16 t = (w == 0) ? acc[0][0] : (w == 1) ? acc[0][1] : (w == 2) ? acc[1][0] : acc[1][1];
#pragma unroll
    for (int slot = 0; slot < 3; ++slot)
#pragma unroll
        for (int g = 0; g < 4; ++g) {
            const f32x4 v = *(LDSP const f32x4*)(L + ((w * 3 + slot) * 4 + g) * 1024 + lane * 16);
            t[4 * g] += v[0]; t[4 * g + 1] += v[1]; t[4 * g + 2] += v[2]; t[4 * g + 3] += v[3];
        }
#pragma unroll
    for (int g = 0; g < 4; ++g) {
        u32x2 ov;
        ov[0] = pack_bf16(t[4 * g] + xv[g][0], t[4 * g + 1] + xv[g][1]);
        ov[1] = pack_bf16(t[4 * g + 2] + xv[g][2], t[4 * g + 3] + xv[g][3]);
        *(u32x2*)(out + base + 8 * g) = ov;
    }
    TS(3, 3);
}

extern "C" void kernel_launch(void* const* d_in, const int* in_sizes, int n_in, void* d_out, int out_size, void* d_ws, size_t ws_size,
                              hipStream_t stream) {
    const u16* x = (const u16*)d_in[0];
    const u16* wq = (const u16*)d_in[1];
    const u16* wk = (const u16*)d_in[2];
    const u16* wv = (const u16*)d_in[3];
    const u16* wo = (const u16*)d_in[4];
    const u16* wg = (const u16*)d_in[6];
    const u16* wu = (const u16*)d_in[7];
    const u16* wd = (const u16*)d_in[8];
    char* ws = (char*)d_ws;
    u16* Qf = (u16*)(ws);
    u16* Kf = (u16*)(ws + (2 << 20));
    u16* Vf = (u16*)(ws + (2 << 20) + 65536);
    float* kmaxp = (float*)(ws + (2 << 20) + 131072);
    unsigned* done = (unsigned*)(ws + (2 << 20) + 196608);
    u16* WoT = (u16*)(ws + (3 << 20));
    u16* HN = (u16*)(ws + (4 << 20));
    float* hbuf = (float*)(ws + (24 << 20));
    u16* HH = (u16*)(ws + (8 << 20));
    u16* WguT = (u16*)(ws + (16 << 20));
    u16* WdT = (u16*)(ws + (20 << 20));
    (void)hipFuncSetAttribute((const void*)k1_ln_qkv, hipFuncAttributeMaxDynamicSharedMemorySize, K1_LDS);
    (void)hipFuncSetAttribute((const void*)k2_attn, hipFuncAttributeMaxDynamicSharedMemorySize, K2_LDS);
    (void)hipFuncSetAttribute((const void*)k45_ffn, hipFuncAttributeMaxDynamicSharedMemorySize, K4_LDS);
    k1_ln_qkv<<<256, 512, K1_LDS, stream>>>(x, wq, wk, wv, wo, wd, Qf, Kf, Vf, kmaxp, WoT, WdT, done);
    k2_attn<<<256, 512, K2_LDS, stream>>>(Qf, Kf, kmaxp, WoT, x, wg, wu, hbuf, HN, WguT);
    k45_ffn<<<256, 256, K4_LDS, stream>>>(HN, WguT, HH, WdT, hbuf, (u16*)d_out, done);
}
```

```cpp
#include <hip/hip_runtime.h>
#include <hip/hip_bf16.h>
#include <stdint.h>
#include <type_traits>

typedef unsigned short u16;
typedef __attribute__((ext_vector_type(8))) short bf16x8;
typedef __attribute__((ext_vector_type(4))) short s16x4;
typedef __attribute__((ext_vector_type(16))) float f32x16;
typedef __attribute__((ext_vector_type(4))) float f32x4;
typedef __attribute__((ext_vector_type(2))) float f32x2;
typedef __attribute__((ext_vector_type(4))) unsigned u32x4;
typedef __attribute__((ext_vector_type(2))) unsigned u32x2;
typedef __attribute__((ext_vector_type(2))) __bf16 bf16x2_t;

#define DI __device__ __forceinline__
#define LDSP __attribute__((address_space(3)))
typedef LDSP const char* lds_cptr;
typedef LDSP char* lds_ptr;

constexpr int S = 2048, D = 512, DF = 1792;
constexpr float LN_EPS = 1e-5f;
constexpr float QSCALE = 0.25f * 1.4426950408889634f;

DI unsigned pack_bf16(float a, float b) { f32x2 v = {a, b}; bf16x2_t r = __builtin_convertvector(v, bf16x2_t); return __builtin_bit_cast(unsigned, r); }
DI float bf_lo(unsigned u) { return __uint_as_float(u << 16); }
DI float bf_hi(unsigned u) { return __uint_as_float(u & 0xffff0000u); }
DI f32x16 mfma32(bf16x8 a, bf16x8 b, f32x16 c) { return __builtin_amdgcn_mfma_f32_32x32x16_bf16(a, b, c, 0, 0, 0); }
DI f32x4 mfma16(bf16x8 a, bf16x8 b, f32x4 c) { return __builtin_amdgcn_mfma_f32_16x16x32_bf16(a, b, c, 0, 0, 0); }
DI void glds16(const void* g, unsigned lds_base) {
    unsigned sv;
    asm volatile("s_mov_b32 %0, m0\n\ts_mov_b32 m0, %2\n\ts_nop 0\n\tglobal_load_lds_dwordx4 %1, off\n\ts_mov_b32 m0, %0"
                 : "=&s"(sv) : "v"(g), "s"(lds_base) : "memory");
}
DI void glds16x4(const void* g, unsigned lds_base) {
    unsigned sv;
    asm volatile("s_mov_b32 %0, m0\n\ts_mov_b32 m0, %2\n\ts_nop 0\n\tglobal_load_lds_dwordx4 %1, off\n\tglobal_load_lds_dwordx4 %1, off offset:1024\n\t"
                 "global_load_lds_dwordx4 %1, off offset:2048\n\tglobal_load_lds_dwordx4 %1, off offset:3072\n\ts_mov_b32 m0, %0"
                 : "=&s"(sv) : "v"(g), "s"(lds_base) : "memory");
}
template <int N> DI void glds_run(const u16* g, unsigned lds_base) {
#pragma unroll
    for (int i = 0; i + 4 <= N; i += 4) glds16x4(g + i * 512, lds_base + i * 1024);
#pragma unroll
    for (int i = N & ~3; i < N; ++i) glds16(g + i * 512, lds_base + i * 1024);
}
DI unsigned rfl(unsigned v) { return (unsigned)__builtin_amdgcn_readfirstlane((int)v); }
DI s16x4 trread(lds_cptr p) { return __builtin_amdgcn_ds_read_tr16_b64_v4i16((LDSP s16x4*)p); }
DI bf16x8 trfrag(lds_cptr p0, lds_cptr p1) { s16x4 a = trread(p0), b = trread(p1); return __builtin_shufflevector(a, b, 0, 1, 2, 3, 4, 5, 6, 7); }
DI bf16x8 ldsfrag(lds_cptr p) { return *(LDSP const bf16x8*)p; }
#define SBAR() __builtin_amdgcn_sched_barrier(0)
#define PIN(x) asm volatile("" : "+v"(x))
#define EX(x) __builtin_amdgcn_exp2f(x)
#define GLD16(dst, ptr, imm) asm volatile("global_load_dwordx4 %0, %1, off offset:%2" : "=v"(dst) : "v"(ptr), "i"(imm))
#define GST16_SC1(ptr, val) asm volatile("global_store_dwordx4 %0, %1, off sc1\n\ts_nop 1" :: "v"(ptr), "v"(val) : "memory")
#define GLD8(dst, ptr, imm) asm volatile("global_load_dwordx2 %0, %1, off offset:%2" : "=v"(dst) : "v"(ptr), "i"(imm))
#define K5P 14
#define PRIO 1
#define ABL2 0
#define ABL4 0
#define TS(k, i)
#define WAIT_VM(N) asm volatile("s_waitcnt vmcnt(" #N ")" ::: "memory")
#define WAIT_VM_BAR(N) asm volatile("s_waitcnt vmcnt(" #N ") lgkmcnt(0)\n\ts_barrier" ::: "memory")

DI void ln_rows8(const u32x4 (&v)[8], int lane, lds_ptr tile, int row0) {
    const int rr = lane >> 3, cc = lane & 7;
    f32x2 f[32];
#pragma unroll
    for (int c = 0; c < 8; ++c)
#pragma unroll
        for (int e = 0; e < 4; ++e) f[4 * c + e] = f32x2{bf_lo(v[c][e]), bf_hi(v[c][e])};
    f32x2 s2 = f[0];
#pragma unroll
    for (int i = 1; i < 32; ++i) s2 += f[i];
    float s = s2[0] + s2[1];
    s += __shfl_xor(s, 1); s += __shfl_xor(s, 2); s += __shfl_xor(s, 4);
    const float mean = s * (1.f / D);
    const f32x2 m2 = {mean, mean};
    f32x2 q2 = {0.f, 0.f};
#pragma unroll
    for (int i = 0; i < 32; ++i) { f[i] -= m2; q2 += f[i] * f[i]; }
    float q = q2[0] + q2[1];
    q += __shfl_xor(q, 1); q += __shfl_xor(q, 2); q += __shfl_xor(q, 4);
    const float rstd = 1.f / sqrtf(q * (1.f / D) + LN_EPS);
    const f32x2 r2 = {rstd, rstd};
    const int row = row0 + rr;
#pragma unroll
    for (int c = 0; c < 8; ++c) {
        u32x4 o;
#pragma unroll
        for (int e = 0; e < 4; ++e) { const f32x2 t = f[4 * c + e] * r2; o[e] = pack_bf16(t[0], t[1]); }
        *(LDSP u32x4*)(tile + row * 1024 + (((cc + 8 * c) ^ (row & 15)) << 4)) = o;
    }
}

constexpr int K1_XN = 0, K1_WQ = 65536, K1_WKV = 131072, K1_ST = 147456, K1_LDS = K1_ST + 9216;
__global__ __launch_bounds__(512, 2) void k1_ln_qkv(const u16* __restrict__ x, const u16* __restrict__ wq, const u16* __restrict__ wk,
                                                  const u16* __restrict__ wv, const u16* __restrict__ wo, const u16* __restrict__ wd,
                                                  u16* __restrict__ Qf, u16* __restrict__ Kf, u16* __restrict__ Vf, float* __restrict__ kmaxp,
                                                  u16* __restrict__ WoT, u16* __restrict__ WdT, unsigned* __restrict__ done) {
    extern __shared__ __attribute__((aligned(16))) char smem[];
    const unsigned lds0 = (unsigned)(uintptr_t)smem;
    lds_ptr L = (lds_ptr)smem;
    const int tid = threadIdx.x, lane = tid & 63, w = (int)rfl(tid >> 6);
    const int bid = blockIdx.x, xcd = bid & 7, idx = bid >> 3, ib = xcd * 4 + (idx >> 3), j = idx & 7;
    const int row0 = ib * 64;
    TS(0, 0);
    if (tid == 0) done[bid] = 0u;
    u32x4 xv[8];
    {
        const u16* p = x + (size_t)(row0 + 8 * w + (lane >> 3)) * D + 8 * (lane & 7);
        GLD16(xv[0], p, 0); GLD16(xv[1], p, 128); GLD16(xv[2], p, 256); GLD16(xv[3], p, 384);
        GLD16(xv[4], p, 512); GLD16(xv[5], p, 640); GLD16(xv[6], p, 768); GLD16(xv[7], p, 896);
    }
    {
        const u16* src = wq + (size_t)((lane >> 2) & 7) * D + 64 * j + 32 * (lane >> 5) + 8 * (lane & 3);
#pragma unroll
        for (int n = 0; n < 8; ++n) { const int nn = w * 8 + n; glds16(src + (size_t)nn * 8 * D, lds0 + K1_WQ + nn * 1024); }
        const u16* s2 = ((j & 1) ? wv : wk) + lane * 8;
#pragma unroll
        for (int n = 0; n < 2; ++n) { const int nn = w * 2 + n; glds16(s2 + nn * 512, lds0 + K1_WKV + nn * 1024); }
        const int nt = bid & 15, sg = bid >> 4;
        {
            const u16* p = w < 7 ? wd + (size_t)(112 * sg + 16 * w + (lane >> 2)) * D + 32 * nt + 8 * (lane & 3)
                                 : wo + (size_t)(32 * sg + (lane >> 2)) * D + 32 * nt + 8 * (lane & 3);
            glds16(p, lds0 + K1_ST + w * 1024);
            if (w == 7) glds16(p + (size_t)16 * D, lds0 + K1_ST + 8 * 1024);
        }
    }
    TS(0, 1);
    WAIT_VM(11);
#pragma unroll
    for (int c = 0; c < 8; ++c) PIN(xv[c]);
    ln_rows8(xv, lane, L + K1_XN, 8 * w);
    TS(0, 2);
    WAIT_VM_BAR(0);
    TS(0, 3);
    const int r = lane & 31, h = lane >> 5, blk = (lane >> 4) & 1, q4 = (lane & 15) >> 2, p4 = lane & 3;
    const int wm = w & 1, wn = (w >> 1) & 1, kh = w >> 2;
    f32x16 acc = {};
    f32x4 akv = {};
    const int trow = 32 * wm + r;
    {
        lds_cptr wb = L + K1_WQ + kh * 32768 + h * 1024 + wn * 512 + q4 * 64 + 32 * blk + 8 * p4;
        lds_cptr xb = L + K1_XN + trow * 1024 + kh * 512;
        bf16x8 fa[16], fb[16];
#pragma unroll
        for (int i = 0; i < 16; ++i) { fa[i] = trfrag(wb + i * 2048, wb + i * 2048 + 256); fb[i] = ldsfrag(xb + ((((2 * i + h) & 15) ^ (trow & 15)) << 4) + (i >> 3) * 256); }
        const int g4 = lane >> 4, c16 = lane & 15, krow = 16 * (j >> 1) + c16;
        bf16x8 wf[2], xf[2];
#pragma unroll
        for (int ss = 0; ss < 2; ++ss) {
            const int s = 2 * w + ss;
            lds_cptr wp = L + K1_WKV + (32 * s + 8 * g4 + q4) * 32 + 8 * p4;
            wf[ss] = trfrag(wp, wp + 128);
            xf[ss] = ldsfrag(L + K1_XN + krow * 1024 + (((4 * s + g4) ^ (krow & 15)) << 4));
        }
#pragma unroll
        for (int i = 0; i < 16; ++i) acc = mfma32(fa[i], fb[i], acc);
#pragma unroll
        for (int ss = 0; ss < 2; ++ss) akv = (j & 1) ? mfma16(xf[ss], wf[ss], akv) : mfma16(wf[ss], xf[ss], akv);
    }
    TS(0, 4);
    {
        const int nt = bid & 15, sg = bid >> 4;
        if (w < 7) {
            lds_cptr p = L + K1_ST + w * 1024 + (4 * h + q4) * 64 + 32 * blk + 8 * p4;
            bf16x8 f = trfrag(p, p + 512);
            *(bf16x8*)(WdT + ((size_t)nt * 112 + 7 * sg + w) * 512 + lane * 8) = f;
        } else {
            const int g4 = lane >> 4;
#pragma unroll
            for (int n = 0; n < 2; ++n) {
                lds_cptr p = L + K1_ST + 7 * 1024 + (8 * g4 + q4) * 64 + 32 * n + 8 * p4;
                bf16x8 f = trfrag(p, p + 256);
                *(bf16x8*)(WoT + ((size_t)(2 * nt + n) * 16 + sg) * 512 + lane * 8) = f;
            }
        }
    }
    asm volatile("s_waitcnt lgkmcnt(0)\n\ts_barrier" ::: "memory");
    {
        LDSP f32x4* ex = (LDSP f32x4*)(L + K1_WQ + w * 2048 + lane * 16);
        const int o = kh ? 0 : 8;
        f32x4 e0, e1;
#pragma unroll
        for (int e = 0; e < 4; ++e) { e0[e] = kh ? acc[e] : acc[8 + e]; e1[e] = kh ? acc[4 + e] : acc[12 + e]; }
        (void)o;
        ex[0] = e0; ex[64] = e1;
        *(LDSP f32x4*)(L + K1_WQ + 16384 + w * 1024 + lane * 16) = akv;
    }
    __syncthreads();
    {
        LDSP const f32x4* ex = (LDSP const f32x4*)(L + K1_WQ + (w ^ 4) * 2048 + lane * 16);
        const f32x4 e0 = ex[0], e1 = ex[64];
        const int grow = row0 + trow;
#pragma unroll
        for (int gg = 0; gg < 2; ++gg) {
            const int g = 2 * kh + gg;
            float v[4];
#pragma unroll
            for (int e = 0; e < 4; ++e) v[e] = ((kh ? acc[8 + 4 * gg + e] : acc[4 * gg + e]) + (gg ? e1[e] : e0[e])) * QSCALE;
            const int head = (64 * j + 32 * wn) / 16 + (g >> 1);
            u32x2 o; o[0] = pack_bf16(v[0], v[1]); o[1] = pack_bf16(v[2], v[3]);
            u16* dst = Qf + ((((size_t)(grow >> 3) * 8 + (head >> 2)) * 2 + (g & 1)) * 32 + (head & 3) * 8 + (grow & 7)) * 8 + 4 * h;
            *(u32x2*)dst = o;
        }
    }
    if (w == 0) {
        const int g4 = lane >> 4, c16 = lane & 15;
        f32x4 t = akv;
#pragma unroll
        for (int ww = 1; ww < 8; ++ww) { f32x4 u = *(LDSP const f32x4*)(L + K1_WQ + 16384 + ww * 1024 + lane * 16); t += u; }
        u32x2 o; o[0] = pack_bf16(t[0], t[1]); o[1] = pack_bf16(t[2], t[3]);
        const int tok0 = row0 + 16 * (j >> 1);
        if (j & 1) {
            const int kb = tok0 >> 5, s = (tok0 >> 4) & 1;
            *(u32x2*)(Vf + ((((size_t)kb * 2 + s) * 2 + (g4 & 1)) * 16 + c16) * 8 + 4 * (g4 >> 1)) = o;
        } else {
            const int key = tok0 + c16;
            *(u32x2*)(Kf + (((size_t)(key >> 5) * 2 + (g4 >> 1)) * 32 + (key & 31)) * 8 + 4 * (g4 & 1)) = o;
            float a = bf_lo(o[0]), b = bf_hi(o[0]), c = bf_lo(o[1]), d = bf_hi(o[1]);
            float n2 = a * a + b * b + c * c + d * d;
            n2 += __shfl_xor(n2, 16); n2 += __shfl_xor(n2, 32);
            n2 = fmaxf(n2, __shfl_xor(n2, 1)); n2 = fmaxf(n2, __shfl_xor(n2, 2)); n2 = fmaxf(n2, __shfl_xor(n2, 4)); n2 = fmaxf(n2, __shfl_xor(n2, 8));
            if (lane == 0) kmaxp[tok0 >> 4] = n2;
        }
    }
    TS(0, 5);
}

constexpr int K2_K = 0, K2_V = 65536, K2_C = 131072, K2_Q = 135168, K2_KM = 143488, K2_ST = 144000, K2_RED = 158336, K2_LDS = 158848;
constexpr int K2_OSTR = 1040;
struct AttnState { f32x16 sA, sB, O; bf16x8 kf, va0, va1, vb0, vb1; lds_cptr kp, vp; };
#define EXP4(SC, o) do { _Pragma("unroll") for (int i = (o); i < (o) + 4; ++i) { SC[i] = EX(SC[i]); } PIN(SC); SBAR(); } while (0)
#define ATT_STEP(SC, SN, V0, V1, W0, W1, KOFF, VOFF) do { \
        u32x4 p0, p1; \
        EXP4(SC, 0); \
        SN = mfma32(kf, qf, cinit); PIN(SN); \
        kf = ldsfrag(kp + (KOFF)); W0 = ldsfrag(vp + (VOFF)); W1 = ldsfrag(vp + (VOFF) + 512); \
        SBAR(); \
        EXP4(SC, 4); \
        EXP4(SC, 8); \
        _Pragma("unroll") for (int i = 0; i < 4; ++i) p0[i] = pack_bf16(SC[2 * i], SC[2 * i + 1]); \
        PIN(p0); SBAR(); \
        O = mfma32(V0, __builtin_bit_cast(bf16x8, p0), O); PIN(O); \
        SBAR(); \
        EXP4(SC, 12); \
        _Pragma("unroll") for (int i = 0; i < 4; ++i) p1[i] = pack_bf16(SC[8 + 2 * i], SC[8 + 2 * i + 1]); \
        PIN(p1); SBAR(); \
        O = mfma32(V1, __builtin_bit_cast(bf16x8, p1), O); PIN(O); \
        SBAR(); \
    } while (0)
#define ATT_RUN(N4) for (int it = 0; it < (N4); ++it) { \
        ATT_STEP(sA, sB, va0, va1, vb0, vb1, 2048, 1024); \
        ATT_STEP(sB, sA, vb0, vb1, va0, va1, 3072, 2048); \
        ATT_STEP(sA, sB, va0, va1, vb0, vb1, 4096, 3072); \
        vp += vinc; \
        ATT_STEP(sB, sA, vb0, vb1, va0, va1, 5120, 0); \
        kp += 4096; }
#define ATT_INIT() do { kp = L + K2_K + lane * 16; vp = vp0; O = f32x16{}; \
        kf = ldsfrag(kp); va0 = ldsfrag(vp); va1 = ldsfrag(vp + 512); sA = mfma32(kf, qf, cinit); kf = ldsfrag(kp + 1024); SBAR(); } while (0)
__global__ __launch_bounds__(512, 2) void k2_attn(const u16* __restrict__ Qf, const u16* __restrict__ KVf,
                                               const float* __restrict__ kmaxp, const u16* __restrict__ WoT, const u16* __restrict__ x,
                                               const u16* __restrict__ wg, const u16* __restrict__ wu,
                                               float* __restrict__ hbuf, u16* __restrict__ HN, u16* __restrict__ WguT) {
    extern __shared__ __attribute__((aligned(16))) char smem[];
    const unsigned lds0 = (unsigned)(uintptr_t)smem;
    lds_ptr L = (lds_ptr)smem;
    const int tid = threadIdx.x, lane = tid & 63, w = (int)rfl(tid >> 6);
    const int rg = blockIdx.x;
    const int r = lane & 31, h = lane >> 5;
    TS(1, 0);
    glds16(Qf + (size_t)rg * 4096 + w * 512 + lane * 8, lds0 + K2_Q + w * 1024);
    if (lane < 32) glds16(kmaxp + lane * 4, lds0 + K2_KM);
    const int kv = (w >> 2) * 64, wq4 = w & 3;
    glds_run<5>(KVf + (size_t)(kv + 5 * wq4) * 512 + lane * 8, lds0 + K2_K + (kv + 5 * wq4) * 1024);
    const u16* kvrest = KVf + (size_t)(kv + 20 + 11 * wq4) * 512 + lane * 8;
    const unsigned kvdst = lds0 + K2_K + (kv + 20 + 11 * wq4) * 1024;
    const int sp = rg & 15, jb = rg >> 4;
    if (tid < 256) {
        const int b = tid >> 5, sl = tid & 31;
        const unsigned val = sl == 0 ? 0x3f803f80u : 0u;
        *(LDSP u32x4*)(L + K2_C + b * 512 + sl * 16) = u32x4{val, val, val, val};
    }
    WAIT_VM_BAR(0);
    TS(1, 1);
    glds_run<4>(kvrest, kvdst);
    const bf16x8 qf = ldsfrag(L + K2_Q + w * 1024 + lane * 16);
    float m;
    {
        float km = fmaxf(*(LDSP const float*)(L + K2_KM + lane * 4), *(LDSP const float*)(L + K2_KM + 256 + lane * 4));
#pragma unroll
        for (int mm = 1; mm < 64; mm <<= 1) km = fmaxf(km, __shfl_xor(km, mm));
        float q2 = 0.f;
        const u32x4 qu = __builtin_bit_cast(u32x4, qf);
#pragma unroll
        for (int e = 0; e < 4; ++e) { float a = bf_lo(qu[e]), b = bf_hi(qu[e]); q2 += a * a + b * b; }
        q2 += __shfl_xor(q2, 32);
        m = sqrtf(q2 * km) * 1.001f + 1e-6f;
    }
    f32x16 cinit;
#pragma unroll
    for (int i = 0; i < 16; ++i) cinit[i] = -m;
    const bool isv = r < 16;
    const int cslot = (r == 16) ? 0 : ((r >= 20 && r < 28) ? 64 : 16);
    const lds_cptr vp0 = isv ? (L + K2_V + h * 256 + r * 16) : (L + K2_C + cslot);
    const int vinc = isv ? 4096 : 0;
    f32x16 sA, sB, O; bf16x8 kf, va0, va1, vb0, vb1; lds_cptr kp, vp;
    if (w >> 2) __builtin_amdgcn_s_setprio(1);
    ATT_INIT();
    ATT_RUN(1);
    glds_run<4>(kvrest + 4 * 512, kvdst + 4096);
    ATT_RUN(1);
    glds_run<3>(kvrest + 8 * 512, kvdst + 8192);
    ATT_RUN(1);
    if (w < 7) {
#pragma unroll
        for (int n = 0; n < 2; ++n) {
            const int c = w * 64 + lane, row = c / 14, ch = c - row * 14;
            glds16((n ? wu : wg) + (size_t)(32 * sp + row) * DF + 112 * jb + 8 * ch, lds0 + K2_ST + n * 7168 + w * 1024);
        }
    }
    ATT_RUN(1);
    TS(1, 2);
    WAIT_VM_BAR(0);
    const int blk = (lane >> 4) & 1, q4 = (lane & 15) >> 2, p4 = lane & 3;
    if (w < 7) {
#pragma unroll
        for (int s2 = 0; s2 < 2; ++s2) {
            lds_cptr p = L + K2_ST + blk * 7168 + (16 * s2 + 8 * h + q4) * 224 + 32 * w + 8 * p4;
            bf16x8 f = trfrag(p, p + 4 * 224);
            *(bf16x8*)(WguT + ((size_t)(7 * jb + w) * 32 + 2 * sp + s2) * 512 + lane * 8) = f;
        }
    }
    const int c16 = lane & 15, g4 = lane >> 4;
    bf16x8 wf[2][16];
    u32x2 xr[4];
    const u16* wp0 = WoT + (size_t)(4 * w) * 16 * 512 + lane * 8;
#define WO_BURST(i0) do { if (ABL2 == 1) { _Pragma("unroll") for (int i = 0; i < 8; ++i) wf[(i0) >> 2][(4 * (i0) + i) & 15] = qf; break; } _Pragma("unroll") for (int i = (i0); i < (i0) + 2; ++i) { const u16* p = wp0 + i * 2048; \
        GLD16(wf[i >> 2][4 * (i & 3)], p, 0); GLD16(wf[i >> 2][4 * (i & 3) + 1], p, 1024); GLD16(wf[i >> 2][4 * (i & 3) + 2], p, 2048); GLD16(wf[i >> 2][4 * (i & 3) + 3], p, 3072); } } while (0)
    WO_BURST(0);
    TS(1, 3);
    ATT_RUN(3);
    WO_BURST(2);
    if (w >> 2) __builtin_amdgcn_s_setprio(0); else __builtin_amdgcn_s_setprio(1);
    ATT_RUN(3);
    asm volatile("s_barrier" ::: "memory");
    if (ABL2 != 1) {
    glds_run<5>(wp0 + (size_t)32 * 512, lds0 + K2_K + 5 * w * 1024);
    glds_run<5>(wp0 + (size_t)37 * 512, lds0 + K2_V + 5 * w * 1024);
    }
    WO_BURST(4);
    ATT_RUN(3);
    WO_BURST(6);
    {
        const u16* xp = x + (size_t)(rg * 8 + (c16 & 7)) * D + 64 * w + 4 * g4;
        GLD8(xr[0], xp, 0); GLD8(xr[1], xp, 32); GLD8(xr[2], xp, 64); GLD8(xr[3], xp, 96);
    }
    ATT_RUN(3);
    TS(1, 4);
    float l = __shfl(O[8], r);
    if (__any(!(l >= 5.9604645e-8f))) {
        asm volatile("s_waitcnt vmcnt(0)" ::: "memory");
        const u16* kg = KVf + lane * 8;
        const u16* vg = KVf + 64 * 512 + h * 128 + (r & 15) * 8;
        const unsigned cv = (r == 16) ? 0x3f803f80u : 0u;
        const u32x4 cst = {cv, cv, cv, cv};
        float mx = -INFINITY;
        const f32x16 z = {};
        for (int kb = 0; kb < 64; ++kb) {
            const f32x16 s = mfma32(*(const bf16x8*)(kg + kb * 512), qf, z);
#pragma unroll
            for (int i = 0; i < 16; ++i) mx = fmaxf(mx, s[i]);
        }
        mx = fmaxf(mx, __shfl_xor(mx, 32));
#pragma unroll
        for (int i = 0; i < 16; ++i) cinit[i] = -mx;
        O = f32x16{};
        for (int kb = 0; kb < 64; ++kb) {
            const f32x16 s = mfma32(*(const bf16x8*)(kg + kb * 512), qf, cinit);
            u32x4 v0 = *(const u32x4*)(vg + kb * 512), v1 = *(const u32x4*)(vg + kb * 512 + 256);
            if (!isv) { v0 = cst; v1 = cst; }
            u32x4 p0, p1;
#pragma unroll
            for (int i = 0; i < 4; ++i) { p0[i] = pack_bf16(EX(s[2 * i]), EX(s[2 * i + 1])); p1[i] = pack_bf16(EX(s[8 + 2 * i]), EX(s[8 + 2 * i + 1])); }
            O = mfma32(__builtin_bit_cast(bf16x8, v0), __builtin_bit_cast(bf16x8, p0), O);
            O = mfma32(__builtin_bit_cast(bf16x8, v1), __builtin_bit_cast(bf16x8, p1), O);
        }
        l = __shfl(O[8], r);
    }
    bf16x8 wl[22];
#pragma unroll
    for (int i = 0; i < 22; ++i) wl[i] = *(const bf16x8*)(wp0 + (size_t)(42 + i) * 512);
    {
        const float inv = 1.f / l;
        lds_ptr dst = L + K2_Q + (r & 7) * K2_OSTR + ((4 * w + (r >> 3)) * 16 + 4 * h) * 2;
        u32x2 a, b;
        a[0] = pack_bf16(O[0] * inv, O[1] * inv); a[1] = pack_bf16(O[2] * inv, O[3] * inv);
        b[0] = pack_bf16(O[4] * inv, O[5] * inv); b[1] = pack_bf16(O[6] * inv, O[7] * inv);
        *(LDSP u32x2*)dst = a; *(LDSP u32x2*)(dst + 16) = b;
    }
    asm volatile("s_waitcnt vmcnt(22) lgkmcnt(0)\n\ts_barrier" ::: "memory");
    TS(1, 5);
#pragma unroll
    for (int i = 0; i < 16; ++i) { PIN(wf[0][i]); PIN(wf[1][i]); }
#pragma unroll
    for (int i = 0; i < 4; ++i) PIN(xr[i]);
    f32x4 acc[4] = {};
    lds_cptr ob = L + K2_Q + (c16 & 7) * K2_OSTR + g4 * 16;
#pragma unroll
    for (int s = 0; s < 16; ++s) {
        const bf16x8 b = ldsfrag(ob + s * 64);
        acc[0] = mfma16(wf[0][s], b, acc[0]);
        acc[1] = mfma16(wf[1][s], b, acc[1]);
        const bf16x8 a2 = s < 10 ? ldsfrag(L + (s < 5 ? K2_K + (5 * w + s) * 1024 : K2_V + (5 * w + s - 5) * 1024) + lane * 16) : wl[s - 10];
        acc[2] = mfma16(a2, b, acc[2]);
        acc[3] = mfma16(wl[6 + s], b, acc[3]);
    }
    PIN(acc[3]);
    TS(1, 6);
    float hf[16];
#pragma unroll
    for (int t = 0; t < 4; ++t) {
        hf[4 * t] = acc[t][0] + bf_lo(xr[t][0]); hf[4 * t + 1] = acc[t][1] + bf_hi(xr[t][0]);
        hf[4 * t + 2] = acc[t][2] + bf_lo(xr[t][1]); hf[4 * t + 3] = acc[t][3] + bf_hi(xr[t][1]);
    }
    float s1 = 0.f;
#pragma unroll
    for (int i = 0; i < 16; ++i) s1 += hf[i];
    s1 += __shfl_xor(s1, 16); s1 += __shfl_xor(s1, 32);
    const float mw = s1 * (1.f / 64.f);
    float m2 = 0.f;
#pragma unroll
    for (int i = 0; i < 16; ++i) { const float d = hf[i] - mw; m2 += d * d; }
    m2 += __shfl_xor(m2, 16); m2 += __shfl_xor(m2, 32);
    if (lane < 8) *(LDSP f32x2*)(L + K2_RED + (w * 8 + lane) * 8) = f32x2{mw, m2};
    const int row = rg * 8 + (c16 & 7);
    asm volatile("s_waitcnt lgkmcnt(0)\n\ts_barrier" ::: "memory");
    float mean = 0.f, M2 = 0.f;
    {
        f32x2 st[8];
#pragma unroll
        for (int ww = 0; ww < 8; ++ww) { st[ww] = *(LDSP const f32x2*)(L + K2_RED + (ww * 8 + (c16 & 7)) * 8); mean += st[ww][0]; }
        mean *= 0.125f;
#pragma unroll
        for (int ww = 0; ww < 8; ++ww) { const float d = st[ww][0] - mean; M2 += st[ww][1] + 64.f * d * d; }
    }
    const float rstd = 1.f / sqrtf(M2 * (1.f / D) + LN_EPS);
    if (c16 < 8) {
#pragma unroll
        for (int t = 0; t < 4; ++t) {
            u32x2 o;
            o[0] = pack_bf16((hf[4 * t] - mean) * rstd, (hf[4 * t + 1] - mean) * rstd);
            o[1] = pack_bf16((hf[4 * t + 2] - mean) * rstd, (hf[4 * t + 3] - mean) * rstd);
            *(u32x2*)(HN + (((size_t)(row >> 5) * 32 + 4 * w + t) * 64 + (g4 >> 1) * 32 + (row & 31)) * 8 + 4 * (g4 & 1)) = o;
        }
#pragma unroll
        for (int t = 0; t < 4; ++t) *(f32x4*)(hbuf + (size_t)row * D + 64 * w + 16 * t + 4 * g4) = f32x4{hf[4 * t], hf[4 * t + 1], hf[4 * t + 2], hf[4 * t + 3]};
    }
    TS(1, 7);
}

constexpr int K4_LDS = 163840;
DI float gelu_tanh(float g) {
    constexpr float c0 = -2.f * 0.7978845608028654f * 1.4426950408889634f, c1 = c0 * 0.044715f;
    const float e = __builtin_amdgcn_exp2f(g * __builtin_fmaf(g * g, c1, c0));
    return g * __builtin_amdgcn_rcpf(1.f + e);
}
template <class Hook>
DI void k4_tile(int bid, const u16* __restrict__ HN, const u16* __restrict__ WguT, u16* __restrict__ HH, char* smem, Hook&& hook) {
    const unsigned lds0 = (unsigned)(uintptr_t)smem;
    lds_ptr L = (lds_ptr)smem;
    const int tid = threadIdx.x, lane = tid & 63, w = (int)rfl(tid >> 6);
    const int xcd = bid & 7, idx = bid >> 3;
    const int ib = (xcd >> 2) * 8 + (idx >> 2), jb = (xcd & 3) * 4 + (idx & 3);
    const int tb = 4 * ib + w;
    TS(2, 0);
    const u16* wsrc = WguT + (size_t)(7 * jb) * 32 * 512 + (size_t)w * 8 * 512 + lane * 8;
    auto issue_tile = [&](int t, int slot) {
        glds16x4(wsrc + (size_t)t * 32 * 512, lds0 + slot * 32768 + w * 8192);
        glds16x4(wsrc + ((size_t)t * 32 + 4) * 512, lds0 + slot * 32768 + w * 8192 + 4096);
    };
    bf16x8 xf[32];
    const u16* hp = HN + (size_t)tb * 32 * 512 + lane * 8;
    const u16* w0 = WguT + (size_t)(7 * jb) * 32 * 512 + (size_t)(2 * w) * 512 + lane * 8;
#define K4_QUARTER(q) do { \
        glds16(w0 + (size_t)(q) * 8 * 512, lds0 + (8 * (q) + 2 * w) * 1024); \
        glds16(w0 + (size_t)(q) * 8 * 512 + 512, lds0 + (8 * (q) + 2 * w + 1) * 1024); \
        _Pragma("unroll") for (int i = 2 * (q); i < 2 * (q) + 2; ++i) { const u16* p = hp + i * 2048; \
            GLD16(xf[4 * i], p, 0); GLD16(xf[4 * i + 1], p, 1024); GLD16(xf[4 * i + 2], p, 2048); GLD16(xf[4 * i + 3], p, 3072); } } while (0)
    K4_QUARTER(0); K4_QUARTER(1);
    f32x16 accp = {};
    bf16x8 fa[2][4];
    WAIT_VM_BAR(10);
#pragma unroll
    for (int i = 0; i < 8; ++i) PIN(xf[i]);
    TS(2, 1);
#pragma unroll
    for (int i = 0; i < 4; ++i) fa[0][i] = ldsfrag(L + lane * 16 + i * 1024);
#pragma unroll
    for (int t = 0; t < 8; ++t) {
        f32x16 acc = {};
        float v[8];
#pragma unroll
        for (int b = 0; b < 8; ++b) {
            if (t == 0) {
                if (b == 0) K4_QUARTER(2);
                if (b == 1) { WAIT_VM_BAR(10); K4_QUARTER(3); }
                if (b == 2) issue_tile(1, 1);
                if (b == 3) { WAIT_VM_BAR(18); issue_tile(2, 2); }
                if (b == 5) { WAIT_VM_BAR(16); issue_tile(3, 3); }
                if (b == 6) issue_tile(4, 4);
                if (b == 7) { WAIT_VM_BAR(24); }
                if ((b & 1) && b < 7) {
#pragma unroll
                    for (int i = 0; i < 8; ++i) PIN(xf[4 * (b + 1) + i]);
                }
            } else if (b == 3) {
                if (t == 1) { WAIT_VM_BAR(0); issue_tile(5, 0); }
                else if (t == 2) { asm volatile("s_waitcnt lgkmcnt(0)\n\ts_barrier" ::: "memory"); issue_tile(6, 1); }
                else if (t == 4) { WAIT_VM_BAR(34); }
            }
            if (t == 3 && b >= 3) hook(std::integral_constant<int, 0>{}, b - 3);
            if (t == 4 && b < 3) hook(std::integral_constant<int, 0>{}, b + 5);
            if (t < 7) {
                if (b < 7 || t < 6) {
                    lds_cptr nb = L + (b < 7 ? (t % 5) * 32768 + 4 * (b + 1) * 1024 : ((t + 1) % 5) * 32768) + lane * 16;
#pragma unroll
                    for (int i = 0; i < 4; ++i) fa[(b + 1) & 1][i] = ldsfrag(nb + i * 1024);
                }
                SBAR();
#pragma unroll
                for (int i = 0; i < 4; ++i) acc = mfma32(fa[b & 1][i], xf[4 * b + i], acc);
            }
            if (t > 0) { v[b] = gelu_tanh(accp[b]) * accp[b + 8]; PIN(v[b]); }
            SBAR();
        }
        if (t > 0) {
            u32x4 o;
            o[0] = pack_bf16(v[0], v[1]); o[1] = pack_bf16(v[2], v[3]); o[2] = pack_bf16(v[4], v[5]); o[3] = pack_bf16(v[6], v[7]);
            GST16_SC1(HH + ((size_t)tb * 112 + 7 * jb + t - 1) * 512 + lane * 8, o);
        }
        accp = acc;
        if (t < 6) TS(2, 2 + t);
    }
#undef K4_QUARTER
}

template <int I, int N, class F> DI void static_for(F&& f) {
    if constexpr (I < N) { f(std::integral_constant<int, I>{}); static_for<I + 1, N>(f); }
}
template <int N> DI void wait_vm() { asm volatile("s_waitcnt vmcnt(%0)" :: "i"(N) : "memory"); }
constexpr int k5_wait(int s, int PV, int PH) {
    int issued = 2 * PV + 2 * PH, pw = s < PV ? 2 * s + 1 : -1, ph = s < PH ? 2 * PV + 2 * s + 1 : -1;
    for (int t = 0; t < s; ++t) {
        if (t + PH < 28) { if (t + PH == s) ph = issued + 1; issued += 2; }
    }
    return issued - 1 - (pw > ph ? pw : ph);
}
#define GLD16_A(dst, ptr, imm) asm volatile("global_load_dwordx4 %0, %1, off offset:%2" : "=a"(dst) : "v"(ptr), "i"(imm))
#define PIN_A(x) asm volatile("" : "+a"(x))
#define GLD16_SC1(dst, ptr, imm) asm volatile("global_load_dwordx4 %0, %1, off offset:%2 sc1" : "=v"(dst) : "v"(ptr), "i"(imm))
__global__ __launch_bounds__(256, 1) void k45_ffn(const u16* __restrict__ HN, const u16* __restrict__ WguT, u16* __restrict__ HH,
                                                  const u16* __restrict__ WdT, const float* __restrict__ hbuf, u16* __restrict__ out,
                                                  unsigned* __restrict__ done) {
    extern __shared__ __attribute__((aligned(16))) char smem[];
    lds_ptr L = (lds_ptr)smem;
    const int tid = threadIdx.x, lane = tid & 63, w = (int)rfl(tid >> 6);
    const int bid = blockIdx.x;
    const int xcd = bid & 7, idx = bid >> 3, ib = xcd * 4 + (idx >> 3), j = idx & 7;
    const u16* a0 = WdT + ((size_t)(2 * j) * 112 + 28 * w) * 512 + lane * 8;
    const u16* a1 = a0 + (size_t)112 * 512;
    const u16* b0 = HH + ((size_t)(2 * ib) * 112 + 28 * w) * 512 + lane * 8;
    const u16* b1 = b0 + (size_t)112 * 512;
    constexpr int PW = 28, PV = 12, PH = 10;
    bf16x8 wa[PW][2];
    int job = bid;
    for (;;) {
        k4_tile(job, HN, WguT, HH, smem, [&](auto, int part) {
            static_for<0, 8>([&](auto pc) {
                constexpr int pp = decltype(pc)::value;
                if (part == pp) {
                    static_for<PV + 2 * pp, PV + 2 * pp + 2>([&](auto ic) {
                        constexpr int s = decltype(ic)::value;
                        const u16* pa = a0 + (s & ~3) * 512; const u16* pb = a1 + (s & ~3) * 512;
                        bf16x8& d0 = wa[s][0]; bf16x8& d1 = wa[s][1];
                        GLD16_A(d0, pa, (s & 3) * 1024); GLD16_A(d1, pb, (s & 3) * 1024);
                    });
                }
            });
        });
        TS(3, 0);
        asm volatile("s_waitcnt vmcnt(0) lgkmcnt(0)\n\ts_barrier" ::: "memory");
        if (tid == 0) {
            const int jx = job & 7, ji = job >> 3;
            const unsigned one = 1u;
            asm volatile("global_store_dword %0, %1, off sc1" :: "v"(done + ((jx >> 2) * 8 + (ji >> 2)) * 16 + (jx & 3) * 4 + (ji & 3)), "v"(one) : "memory");
        }
        static_for<0, PV>([&](auto ic) {
            constexpr int s = decltype(ic)::value;
            const u16* pa = a0 + (s & ~3) * 512; const u16* pb = a1 + (s & ~3) * 512;
            bf16x8& d0 = wa[s][0]; bf16x8& d1 = wa[s][1];
            GLD16(d0, pa, (s & 3) * 1024); GLD16(d1, pb, (s & 3) * 1024);
        });
        LDSP int* miss = (LDSP int*)L;
        if (w == 0) {
            int m = -1;
            const unsigned* fl = done + (ib >> 1) * 16 + (lane & 15);
            for (int spin = 0; spin < 4096; ++spin) {
                unsigned f;
                asm volatile("global_load_dword %0, %1, off sc1\n\ts_waitcnt vmcnt(0)" : "=v"(f) : "v"(fl) : "memory");
                const unsigned long long bad = __ballot(f == 0u) & 0xffffull;
                m = bad ? (int)__builtin_ctzll(bad) : -1;
                if (m < 0) break;
                __builtin_amdgcn_s_sleep(2);
            }
            if (lane == 0) *miss = m;
        }
        asm volatile("s_waitcnt lgkmcnt(0)\n\ts_barrier" ::: "memory");
        const int m = (int)rfl((unsigned)*miss);
        if (m < 0) break;
        asm volatile("s_waitcnt vmcnt(0) lgkmcnt(0)\n\ts_barrier" ::: "memory");
        const int ibp = ib >> 1, jbp = m;
        job = ((ibp & 7) * 4 + (jbp & 3)) * 8 + ((ibp >> 3) * 4 + (jbp >> 2));
    }
    TS(3, 1);
    const int r = lane & 31, h = lane >> 5;
    const int na = w >> 1, tbq = w & 1;
    const size_t base = (size_t)(ib * 64 + 32 * tbq + r) * D + 64 * j + 32 * na + 4 * h;
    f32x4 xv[4];
    { const float* hp = hbuf + base; GLD16(xv[0], hp, 0); GLD16(xv[1], hp, 32); GLD16(xv[2], hp, 64); GLD16(xv[3], hp, 96); }
    bf16x8 hb[PH][2];
#pragma unroll
    for (int s = 0; s < PH; ++s) { GLD16_SC1(hb[s][0], b0 + s * 512, 0); GLD16_SC1(hb[s][1], b1 + s * 512, 0); }
    f32x16 acc[2][2] = {};
    static_for<0, 28>([&](auto ic) {
        constexpr int s = decltype(ic)::value, sw = s % PW, sh = s % PH;
        wait_vm<k5_wait(s, PV, PH)>();
        bf16x8& w0 = wa[sw][0]; bf16x8& w1 = wa[sw][1]; bf16x8& h0 = hb[sh][0]; bf16x8& h1 = hb[sh][1];
        if constexpr (s < PV) { PIN(w0); PIN(w1); } else { PIN_A(w0); PIN_A(w1); }
        PIN(h0); PIN(h1);
        acc[0][0] = mfma32(wa[sw][0], hb[sh][0], acc[0][0]);
        acc[0][1] = mfma32(wa[sw][0], hb[sh][1], acc[0][1]);
        acc[1][0] = mfma32(wa[sw][1], hb[sh][0], acc[1][0]);
        acc[1][1] = mfma32(wa[sw][1], hb[sh][1], acc[1][1]);
        SBAR();
        const u16* pb0 = b0 + (s + PH) * 512; const u16* pb1 = b1 + (s + PH) * 512;
        if constexpr (s + PH < 28) { GLD16_SC1(h0, pb0, 0); GLD16_SC1(h1, pb1, 0); }
        SBAR();
    });
    TS(3, 2);
#pragma unroll
    for (int q = 0; q < 4; ++q) {
        if (q != w) {
            const int slot = (w - q + 3) & 3;
#pragma unroll
            for (int g = 0; g < 4; ++g) {
                f32x4 v = {acc[q >> 1][q & 1][4 * g], acc[q >> 1][q & 1][4 * g + 1], acc[q >> 1][q & 1][4 * g + 2], acc[q >> 1][q & 1][4 * g + 3]};
                *(LDSP f32x4*)(L + ((q * 3 + slot) * 4 + g) * 1024 + lane * 16) = v;
            }
        }
    }
    asm volatile("s_waitcnt lgkmcnt(0)\n\ts_barrier" ::: "memory");
#pragma unroll
    for (int g = 0; g < 4; ++g) PIN(xv[g]);
    f32x16 t = (w == 0) ? acc[0][0] : (w == 1) ? acc[0][1] : (w == 2) ? acc[1][0] : acc[1][1];
#pragma unroll
    for (int slot = 0; slot < 3; ++slot)
#pragma unroll
        for (int g = 0; g < 4; ++g) {
            const f32x4 v = *(LDSP const f32x4*)(L + ((w * 3 + slot) * 4 + g) * 1024 + lane * 16);
            t[4 * g] += v[0]; t[4 * g + 1] += v[1]; t[4 * g + 2] += v[2]; t[4 * g + 3] += v[3];
        }
#pragma unroll
    for (int g = 0; g < 4; ++g) {
        u32x2 ov;
        ov[0] = pack_bf16(t[4 * g] + xv[g][0], t[4 * g + 1] + xv[g][1]);
        ov[1] = pack_bf16(t[4 * g + 2] + xv[g][2], t[4 * g + 3] + xv[g][3]);
        *(u32x2*)(out + base + 8 * g) = ov;
    }
    TS(3, 3);
}

extern "C" void kernel_launch(void* const* d_in, const int* in_sizes, int n_in, void* d_out, int out_size, void* d_ws, size_t ws_size,
                              hipStream_t stream) {
    const u16* x = (const u16*)d_in[0];
    const u16* wq = (const u16*)d_in[1];
    const u16* wk = (const u16*)d_in[2];
    const u16* wv = (const u16*)d_in[3];
    const u16* wo = (const u16*)d_in[4];
    const u16* wg = (const u16*)d_in[6];
    const u16* wu = (const u16*)d_in[7];
    const u16* wd = (const u16*)d_in[8];
    char* ws = (char*)d_ws;
    u16* Qf = (u16*)(ws);
    u16* Kf = (u16*)(ws + (2 << 20));
    u16* Vf = (u16*)(ws + (2 << 20) + 65536);
    float* kmaxp = (float*)(ws + (2 << 20) + 131072);
    unsigned* done = (unsigned*)(ws + (2 << 20) + 196608);
    u16* WoT = (u16*)(ws + (3 << 20));
    u16* HN = (u16*)(ws + (4 << 20));
    float* hbuf = (float*)(ws + (24 << 20));
    u16* HH = (u16*)(ws + (8 << 20));
    u16* WguT = (u16*)(ws + (16 << 20));
    u16* WdT = (u16*)(ws + (20 << 20));
    (void)hipFuncSetAttribute((const void*)k1_ln_qkv, hipFuncAttributeMaxDynamicSharedMemorySize, K1_LDS);
    (void)hipFuncSetAttribute((const void*)k2_attn, hipFuncAttributeMaxDynamicSharedMemorySize, K2_LDS);
    (void)hipFuncSetAttribute((const void*)k45_ffn, hipFuncAttributeMaxDynamicSharedMemorySize, K4_LDS);
    k1_ln_qkv<<<256, 512, K1_LDS, stream>>>(x, wq, wk, wv, wo, wd, Qf, Kf, Vf, kmaxp, WoT, WdT, done);
    k2_attn<<<256, 512, K2_LDS, stream>>>(Qf, Kf, kmaxp, WoT, x, wg, wu, hbuf, HN, WguT);
    k45_ffn<<<256, 256, K4_LDS, stream>>>(HN, WguT, HH, WdT, hbuf, (u16*)d_out, done);
}
```

```cpp
#include <hip/hip_runtime.h>
#include <hip/hip_bf16.h>
#include <stdint.h>
#include <type_traits>

typedef unsigned short u16;
typedef __attribute__((ext_vector_type(8))) short bf16x8;
typedef __attribute__((ext_vector_type(4))) short s16x4;
typedef __attribute__((ext_vector_type(16))) float f32x16;
typedef __attribute__((ext_vector_type(4))) float f32x4;
typedef __attribute__((ext_vector_type(2))) float f32x2;
typedef __attribute__((ext_vector_type(4))) unsigned u32x4;
typedef __attribute__((ext_vector_type(2))) unsigned u32x2;
typedef __attribute__((ext_vector_type(2))) __bf16 bf16x2_t;

#define DI __device__ __forceinline__
#define LDSP __attribute__((address_space(3)))
typedef LDSP const char* lds_cptr;
typedef LDSP char* lds_ptr;

constexpr int S = 2048, D = 512, DF = 1792;
constexpr float LN_EPS = 1e-5f;
constexpr float QSCALE = 0.25f * 1.4426950408889634f;

DI unsigned pack_bf16(float a, float b) { f32x2 v = {a, b}; bf16x2_t r = __builtin_convertvector(v, bf16x2_t); return __builtin_bit_cast(unsigned, r); }
DI float bf_lo(unsigned u) { return __uint_as_float(u << 16); }
DI float bf_hi(unsigned u) { return __uint_as_float(u & 0xffff0000u); }
DI f32x16 mfma32(bf16x8 a, bf16x8 b, f32x16 c) { return __builtin_amdgcn_mfma_f32_32x32x16_bf16(a, b, c, 0, 0, 0); }
DI f32x4 mfma16(bf16x8 a, bf16x8 b, f32x4 c) { return __builtin_amdgcn_mfma_f32_16x16x32_bf16(a, b, c, 0, 0, 0); }
DI void glds16(const void* g, unsigned lds_base) {
    unsigned sv;
    asm volatile("s_mov_b32 %0, m0\n\ts_mov_b32 m0, %2\n\ts_nop 0\n\tglobal_load_lds_dwordx4 %1, off\n\ts_mov_b32 m0, %0"
                 : "=&s"(sv) : "v"(g), "s"(lds_base) : "memory");
}
DI void glds16x4(const void* g, unsigned lds_base) {
    unsigned sv;
    asm volatile("s_mov_b32 %0, m0\n\ts_mov_b32 m0, %2\n\ts_nop 0\n\tglobal_load_lds_dwordx4 %1, off\n\tglobal_load_lds_dwordx4 %1, off offset:1024\n\t"
                 "global_load_lds_dwordx4 %1, off offset:2048\n\tglobal_load_lds_dwordx4 %1, off offset:3072\n\ts_mov_b32 m0, %0"
                 : "=&s"(sv) : "v"(g), "s"(lds_base) : "memory");
}
template <int N> DI void glds_run(const u16* g, unsigned lds_base) {
#pragma unroll
    for (int i = 0; i + 4 <= N; i += 4) glds16x4(g + i * 512, lds_base + i * 1024);
#pragma unroll
    for (int i = N & ~3; i < N; ++i) glds16(g + i * 512, lds_base + i * 1024);
}
DI unsigned rfl(unsigned v) { return (unsigned)__builtin_amdgcn_readfirstlane((int)v); }
DI s16x4 trread(lds_cptr p) { return __builtin_amdgcn_ds_read_tr16_b64_v4i16((LDSP s16x4*)p); }
DI bf16x8 trfrag(lds_cptr p0, lds_cptr p1) { s16x4 a = trread(p0), b = trread(p1); return __builtin_shufflevector(a, b, 0, 1, 2, 3, 4, 5, 6, 7); }
DI bf16x8 ldsfrag(lds_cptr p) { return *(LDSP const bf16x8*)p; }
#define SBAR() __builtin_amdgcn_sched_barrier(0)
#define PIN(x) asm volatile("" : "+v"(x))
#define EX(x) __builtin_amdgcn_exp2f(x)
#define GLD16(dst, ptr, imm) asm volatile("global_load_dwordx4 %0, %1, off offset:%2" : "=v"(dst) : "v"(ptr), "i"(imm))
#define GST16_SC1(ptr, val) asm volatile("global_store_dwordx4 %0, %1, off sc1\n\ts_nop 1" :: "v"(ptr), "v"(val) : "memory")
#define GLD8(dst, ptr, imm) asm volatile("global_load_dwordx2 %0, %1, off offset:%2" : "=v"(dst) : "v"(ptr), "i"(imm))
#define K5P 14
#define PRIO 1
#define ABL2 0
#define ABL4 0
#define TS(k, i)
#define WAIT_VM(N) asm volatile("s_waitcnt vmcnt(" #N ")" ::: "memory")
#define WAIT_VM_BAR(N) asm volatile("s_waitcnt vmcnt(" #N ") lgkmcnt(0)\n\ts_barrier" ::: "memory")

template <int CTRL> DI float dpp(float v) { return __int_as_float(__builtin_amdgcn_update_dpp(0, __float_as_int(v), CTRL, 0xf, 0xf, true)); }
DI float sum8(float v) { v += dpp<0xB1>(v); v += dpp<0x4E>(v); v += dpp<0x141>(v); return v; }
DI float max16(float v) { v = fmaxf(v, dpp<0xB1>(v)); v = fmaxf(v, dpp<0x4E>(v)); v = fmaxf(v, dpp<0x141>(v)); v = fmaxf(v, dpp<0x140>(v)); return v; }
DI float sum_x16(float v) { auto r = __builtin_amdgcn_permlane16_swap(__float_as_uint(v), __float_as_uint(v), false, false); return __uint_as_float(r[0]) + __uint_as_float(r[1]); }
DI float sum_x32(float v) { auto r = __builtin_amdgcn_permlane32_swap(__float_as_uint(v), __float_as_uint(v), false, false); return __uint_as_float(r[0]) + __uint_as_float(r[1]); }
DI float max_x16(float v) { auto r = __builtin_amdgcn_permlane16_swap(__float_as_uint(v), __float_as_uint(v), false, false); return fmaxf(__uint_as_float(r[0]), __uint_as_float(r[1])); }
DI float max_x32(float v) { auto r = __builtin_amdgcn_permlane32_swap(__float_as_uint(v), __float_as_uint(v), false, false); return fmaxf(__uint_as_float(r[0]), __uint_as_float(r[1])); }

DI void ln_rows8(const u32x4 (&v)[8], int lane, lds_ptr tile, int row0) {
    const int rr = lane >> 3, cc = lane & 7;
    f32x2 f[32];
#pragma unroll
    for (int c = 0; c < 8; ++c)
#pragma unroll
        for (int e = 0; e < 4; ++e) f[4 * c + e] = f32x2{bf_lo(v[c][e]), bf_hi(v[c][e])};
    f32x2 s2 = f[0];
#pragma unroll
    for (int i = 1; i < 32; ++i) s2 += f[i];
    float s = s2[0] + s2[1];
    s = sum8(s);
    const float mean = s * (1.f / D);
    const f32x2 m2 = {mean, mean};
    f32x2 q2 = {0.f, 0.f};
#pragma unroll
    for (int i = 0; i < 32; ++i) { f[i] -= m2; q2 += f[i] * f[i]; }
    float q = q2[0] + q2[1];
    q = sum8(q);
    const float rstd = __builtin_amdgcn_rsqf(q * (1.f / D) + LN_EPS);
    const f32x2 r2 = {rstd, rstd};
    const int row = row0 + rr;
#pragma unroll
    for (int c = 0; c < 8; ++c) {
        u32x4 o;
#pragma unroll
        for (int e = 0; e < 4; ++e) { const f32x2 t = f[4 * c + e] * r2; o[e] = pack_bf16(t[0], t[1]); }
        *(LDSP u32x4*)(tile + row * 1024 + (((cc + 8 * c) ^ (row & 15)) << 4)) = o;
    }
}

constexpr int K1_XN = 0, K1_WQ = 65536, K1_WKV = 131072, K1_ST = 147456, K1_LDS = K1_ST + 9216;
__global__ __launch_bounds__(512, 2) void k1_ln_qkv(const u16* __restrict__ x, const u16* __restrict__ wq, const u16* __restrict__ wk,
                                                  const u16* __restrict__ wv, const u16* __restrict__ wo, const u16* __restrict__ wd,
                                                  u16* __restrict__ Qf, u16* __restrict__ Kf, u16* __restrict__ Vf, float* __restrict__ kmaxp,
                                                  u16* __restrict__ WoT, u16* __restrict__ WdT, unsigned* __restrict__ done) {
    extern __shared__ __attribute__((aligned(16))) char smem[];
    const unsigned lds0 = (unsigned)(uintptr_t)smem;
    lds_ptr L = (lds_ptr)smem;
    const int tid = threadIdx.x, lane = tid & 63, w = (int)rfl(tid >> 6);
    const int bid = blockIdx.x, xcd = bid & 7, idx = bid >> 3, ib = xcd * 4 + (idx >> 3), j = idx & 7;
    const int row0 = ib * 64;
    TS(0, 0);
    if (tid == 0) done[bid] = 0u;
    u32x4 xv[8];
    {
        const u16* p = x + (size_t)(row0 + 8 * w + (lane >> 3)) * D + 8 * (lane & 7);
        GLD16(xv[0], p, 0); GLD16(xv[1], p, 128); GLD16(xv[2], p, 256); GLD16(xv[3], p, 384);
        GLD16(xv[4], p, 512); GLD16(xv[5], p, 640); GLD16(xv[6], p, 768); GLD16(xv[7], p, 896);
    }
    {
        const u16* src = wq + (size_t)((lane >> 2) & 7) * D + 64 * j + 32 * (lane >> 5) + 8 * (lane & 3);
#pragma unroll
        for (int n = 0; n < 8; ++n) { const int nn = w * 8 + n; glds16(src + (size_t)nn * 8 * D, lds0 + K1_WQ + nn * 1024); }
        const u16* s2 = ((j & 1) ? wv : wk) + lane * 8;
#pragma unroll
        for (int n = 0; n < 2; ++n) { const int nn = w * 2 + n; glds16(s2 + nn * 512, lds0 + K1_WKV + nn * 1024); }
        const int nt = bid & 15, sg = bid >> 4;
        {
            const u16* p = w < 7 ? wd + (size_t)(112 * sg + 16 * w + (lane >> 2)) * D + 32 * nt + 8 * (lane & 3)
                                 : wo + (size_t)(32 * sg + (lane >> 2)) * D + 32 * nt + 8 * (lane & 3);
            glds16(p, lds0 + K1_ST + w * 1024);
            if (w == 7) glds16(p + (size_t)16 * D, lds0 + K1_ST + 8 * 1024);
        }
    }
    TS(0, 1);
    WAIT_VM(11);
#pragma unroll
    for (int c = 0; c < 8; ++c) PIN(xv[c]);
    ln_rows8(xv, lane, L + K1_XN, 8 * w);
    TS(0, 2);
    WAIT_VM_BAR(0);
    TS(0, 3);
    const int r = lane & 31, h = lane >> 5, blk = (lane >> 4) & 1, q4 = (lane & 15) >> 2, p4 = lane & 3;
    const int wm = w & 1, wn = (w >> 1) & 1, kh = w >> 2;
    f32x16 acc = {};
    f32x4 akv = {};
    const int trow = 32 * wm + r;
    {
        lds_cptr wb = L + K1_WQ + kh * 32768 + h * 1024 + wn * 512 + q4 * 64 + 32 * blk + 8 * p4;
        lds_cptr xb = L + K1_XN + trow * 1024 + kh * 512;
        bf16x8 fa[16], fb[16];
#pragma unroll
        for (int i = 0; i < 16; ++i) { fa[i] = trfrag(wb + i * 2048, wb + i * 2048 + 256); fb[i] = ldsfrag(xb + ((((2 * i + h) & 15) ^ (trow & 15)) << 4) + (i >> 3) * 256); }
        const int g4 = lane >> 4, c16 = lane & 15, krow = 16 * (j >> 1) + c16;
        bf16x8 wf[2], xf[2];
#pragma unroll
        for (int ss = 0; ss < 2; ++ss) {
            const int s = 2 * w + ss;
            lds_cptr wp = L + K1_WKV + (32 * s + 8 * g4 + q4) * 32 + 8 * p4;
            wf[ss] = trfrag(wp, wp + 128);
            xf[ss] = ldsfrag(L + K1_XN + krow * 1024 + (((4 * s + g4) ^ (krow & 15)) << 4));
        }
#pragma unroll
        for (int i = 0; i < 16; ++i) acc = mfma32(fa[i], fb[i], acc);
#pragma unroll
        for (int ss = 0; ss < 2; ++ss) akv = (j & 1) ? mfma16(xf[ss], wf[ss], akv) : mfma16(wf[ss], xf[ss], akv);
    }
    TS(0, 4);
    {
        const int nt = bid & 15, sg = bid >> 4;
        if (w < 7) {
            lds_cptr p = L + K1_ST + w * 1024 + (4 * h + q4) * 64 + 32 * blk + 8 * p4;
            bf16x8 f = trfrag(p, p + 512);
            *(bf16x8*)(WdT + ((size_t)nt * 112 + 7 * sg + w) * 512 + lane * 8) = f;
        } else {
            const int g4 = lane >> 4;
#pragma unroll
            for (int n = 0; n < 2; ++n) {
                lds_cptr p = L + K1_ST + 7 * 1024 + (8 * g4 + q4) * 64 + 32 * n + 8 * p4;
                bf16x8 f = trfrag(p, p + 256);
                *(bf16x8*)(WoT + ((size_t)(2 * nt + n) * 16 + sg) * 512 + lane * 8) = f;
            }
        }
    }
    asm volatile("s_waitcnt lgkmcnt(0)\n\ts_barrier" ::: "memory");
    {
        LDSP f32x4* ex = (LDSP f32x4*)(L + K1_WQ + w * 2048 + lane * 16);
        const int o = kh ? 0 : 8;
        f32x4 e0, e1;
#pragma unroll
        for (int e = 0; e < 4; ++e) { e0[e] = kh ? acc[e] : acc[8 + e]; e1[e] = kh ? acc[4 + e] : acc[12 + e]; }
        (void)o;
        ex[0] = e0; ex[64] = e1;
        *(LDSP f32x4*)(L + K1_WQ + 16384 + w * 1024 + lane * 16) = akv;
    }
    __syncthreads();
    {
        LDSP const f32x4* ex = (LDSP const f32x4*)(L + K1_WQ + (w ^ 4) * 2048 + lane * 16);
        const f32x4 e0 = ex[0], e1 = ex[64];
        const int grow = row0 + trow;
#pragma unroll
        for (int gg = 0; gg < 2; ++gg) {
            const int g = 2 * kh + gg;
            float v[4];
#pragma unroll
            for (int e = 0; e < 4; ++e) v[e] = ((kh ? acc[8 + 4 * gg + e] : acc[4 * gg + e]) + (gg ? e1[e] : e0[e])) * QSCALE;
            const int head = (64 * j + 32 * wn) / 16 + (g >> 1);
            u32x2 o; o[0] = pack_bf16(v[0], v[1]); o[1] = pack_bf16(v[2], v[3]);
            u16* dst = Qf + ((((size_t)(grow >> 3) * 8 + (head >> 2)) * 2 + (g & 1)) * 32 + (head & 3) * 8 + (grow & 7)) * 8 + 4 * h;
            *(u32x2*)dst = o;
        }
    }
    if (w == 0) {
        const int g4 = lane >> 4, c16 = lane & 15;
        f32x4 t = akv;
#pragma unroll
        for (int ww = 1; ww < 8; ++ww) { f32x4 u = *(LDSP const f32x4*)(L + K1_WQ + 16384 + ww * 1024 + lane * 16); t += u; }
        u32x2 o; o[0] = pack_bf16(t[0], t[1]); o[1] = pack_bf16(t[2], t[3]);
        const int tok0 = row0 + 16 * (j >> 1);
        if (j & 1) {
            const int kb = tok0 >> 5, s = (tok0 >> 4) & 1;
            *(u32x2*)(Vf + ((((size_t)kb * 2 + s) * 2 + (g4 & 1)) * 16 + c16) * 8 + 4 * (g4 >> 1)) = o;
        } else {
            const int key = tok0 + c16;
            *(u32x2*)(Kf + (((size_t)(key >> 5) * 2 + (g4 >> 1)) * 32 + (key & 31)) * 8 + 4 * (g4 & 1)) = o;
            float a = bf_lo(o[0]), b = bf_hi(o[0]), c = bf_lo(o[1]), d = bf_hi(o[1]);
            float n2 = a * a + b * b + c * c + d * d;
            n2 = max16(sum_x32(sum_x16(n2)));
            if (lane == 0) kmaxp[tok0 >> 4] = n2;
        }
    }
    TS(0, 5);
}

constexpr int K2_K = 0, K2_V = 65536, K2_C = 131072, K2_Q = 135168, K2_KM = 143488, K2_ST = 144000, K2_RED = 158336, K2_LDS = 158848;
constexpr int K2_OSTR = 1040;
struct AttnState { f32x16 sA, sB, O; bf16x8 kf, va0, va1, vb0, vb1; lds_cptr kp, vp; };
#define EXP4(SC, o) do { _Pragma("unroll") for (int i = (o); i < (o) + 4; ++i) { SC[i] = EX(SC[i]); } PIN(SC); SBAR(); } while (0)
#define ATT_STEP(SC, SN, V0, V1, W0, W1, KOFF, VOFF) do { \
        u32x4 p0, p1; \
        EXP4(SC, 0); \
        SN = mfma32(kf, qf, cinit); PIN(SN); \
        kf = ldsfrag(kp + (KOFF)); W0 = ldsfrag(vp + (VOFF)); W1 = ldsfrag(vp + (VOFF) + 512); \
        SBAR(); \
        EXP4(SC, 4); \
        EXP4(SC, 8); \
        _Pragma("unroll") for (int i = 0; i < 4; ++i) p0[i] = pack_bf16(SC[2 * i], SC[2 * i + 1]); \
        PIN(p0); SBAR(); \
        O = mfma32(V0, __builtin_bit_cast(bf16x8, p0), O); PIN(O); \
        SBAR(); \
        EXP4(SC, 12); \
        _Pragma("unroll") for (int i = 0; i < 4; ++i) p1[i] = pack_bf16(SC[8 + 2 * i], SC[8 + 2 * i + 1]); \
        PIN(p1); SBAR(); \
        O = mfma32(V1, __builtin_bit_cast(bf16x8, p1), O); PIN(O); \
        SBAR(); \
    } while (0)
#define ATT_RUN(N4) for (int it = 0; it < (N4); ++it) { \
        ATT_STEP(sA, sB, va0, va1, vb0, vb1, 2048, 1024); \
        ATT_STEP(sB, sA, vb0, vb1, va0, va1, 3072, 2048); \
        ATT_STEP(sA, sB, va0, va1, vb0, vb1, 4096, 3072); \
        vp += vinc; \
        ATT_STEP(sB, sA, vb0, vb1, va0, va1, 5120, 0); \
        kp += 4096; }
#define ATT_INIT() do { kp = L + K2_K + lane * 16; vp = vp0; O = f32x16{}; \
        kf = ldsfrag(kp); va0 = ldsfrag(vp); va1 = ldsfrag(vp + 512); sA = mfma32(kf, qf, cinit); kf = ldsfrag(kp + 1024); SBAR(); } while (0)
__global__ __launch_bounds__(512, 2) void k2_attn(const u16* __restrict__ Qf, const u16* __restrict__ KVf,
                                               const float* __restrict__ kmaxp, const u16* __restrict__ WoT, const u16* __restrict__ x,
                                               const u16* __restrict__ wg, const u16* __restrict__ wu,
                                               float* __restrict__ hbuf, u16* __restrict__ HN, u16* __restrict__ WguT) {
    extern __shared__ __attribute__((aligned(16))) char smem[];
    const unsigned lds0 = (unsigned)(uintptr_t)smem;
    lds_ptr L = (lds_ptr)smem;
    const int tid = threadIdx.x, lane = tid & 63, w = (int)rfl(tid >> 6);
    const int rg = blockIdx.x;
    const int r = lane & 31, h = lane >> 5;
    TS(1, 0);
    glds16(Qf + (size_t)rg * 4096 + w * 512 + lane * 8, lds0 + K2_Q + w * 1024);
    if (lane < 32) glds16(kmaxp + lane * 4, lds0 + K2_KM);
    const int kv = (w >> 2) * 64, wq4 = w & 3;
    glds_run<5>(KVf + (size_t)(kv + 5 * wq4) * 512 + lane * 8, lds0 + K2_K + (kv + 5 * wq4) * 1024);
    const u16* kvrest = KVf + (size_t)(kv + 20 + 11 * wq4) * 512 + lane * 8;
    const unsigned kvdst = lds0 + K2_K + (kv + 20 + 11 * wq4) * 1024;
    const int sp = rg & 15, jb = rg >> 4;
    if (tid < 256) {
        const int b = tid >> 5, sl = tid & 31;
        const unsigned val = sl == 0 ? 0x3f803f80u : 0u;
        *(LDSP u32x4*)(L + K2_C + b * 512 + sl * 16) = u32x4{val, val, val, val};
    }
    WAIT_VM_BAR(0);
    TS(1, 1);
    glds_run<4>(kvrest, kvdst);
    const bf16x8 qf = ldsfrag(L + K2_Q + w * 1024 + lane * 16);
    float m;
    {
        float km = fmaxf(*(LDSP const float*)(L + K2_KM + lane * 4), *(LDSP const float*)(L + K2_KM + 256 + lane * 4));
        km = max_x32(max_x16(max16(km)));
        float q2 = 0.f;
        const u32x4 qu = __builtin_bit_cast(u32x4, qf);
#pragma unroll
        for (int e = 0; e < 4; ++e) { float a = bf_lo(qu[e]), b = bf_hi(qu[e]); q2 += a * a + b * b; }
        q2 = sum_x32(q2);
        m = __builtin_amdgcn_sqrtf(q2 * km) * 1.001f + 1e-6f;
    }
    f32x16 cinit;
#pragma unroll
    for (int i = 0; i < 16; ++i) cinit[i] = -m;
    const bool isv = r < 16;
    const int cslot = (r == 16) ? 0 : ((r >= 20 && r < 28) ? 64 : 16);
    const lds_cptr vp0 = isv ? (L + K2_V + h * 256 + r * 16) : (L + K2_C + cslot);
    const int vinc = isv ? 4096 : 0;
    f32x16 sA, sB, O; bf16x8 kf, va0, va1, vb0, vb1; lds_cptr kp, vp;
    if (w >> 2) __builtin_amdgcn_s_setprio(1);
    ATT_INIT();
    ATT_RUN(1);
    glds_run<4>(kvrest + 4 * 512, kvdst + 4096);
    ATT_RUN(1);
    glds_run<3>(kvrest + 8 * 512, kvdst + 8192);
    ATT_RUN(1);
    if (w < 7) {
#pragma unroll
        for (int n = 0; n < 2; ++n) {
            const int c = w * 64 + lane, row = c / 14, ch = c - row * 14;
            glds16((n ? wu : wg) + (size_t)(32 * sp + row) * DF + 112 * jb + 8 * ch, lds0 + K2_ST + n * 7168 + w * 1024);
        }
    }
    ATT_RUN(1);
    TS(1, 2);
    WAIT_VM_BAR(0);
    const int blk = (lane >> 4) & 1, q4 = (lane & 15) >> 2, p4 = lane & 3;
    if (w < 7) {
#pragma unroll
        for (int s2 = 0; s2 < 2; ++s2) {
            lds_cptr p = L + K2_ST + blk * 7168 + (16 * s2 + 8 * h + q4) * 224 + 32 * w + 8 * p4;
            bf16x8 f = trfrag(p, p + 4 * 224);
            *(bf16x8*)(WguT + ((size_t)(7 * jb + w) * 32 + 2 * sp + s2) * 512 + lane * 8) = f;
        }
    }
    const int c16 = lane & 15, g4 = lane >> 4;
    bf16x8 wf[2][16];
    u32x2 xr[4];
    const u16* wp0 = WoT + (size_t)(4 * w) * 16 * 512 + lane * 8;
#define WO_BURST(i0) do { if (ABL2 == 1) { _Pragma("unroll") for (int i = 0; i < 8; ++i) wf[(i0) >> 2][(4 * (i0) + i) & 15] = qf; break; } _Pragma("unroll") for (int i = (i0); i < (i0) + 2; ++i) { const u16* p = wp0 + i * 2048; \
        GLD16(wf[i >> 2][4 * (i & 3)], p, 0); GLD16(wf[i >> 2][4 * (i & 3) + 1], p, 1024); GLD16(wf[i >> 2][4 * (i & 3) + 2], p, 2048); GLD16(wf[i >> 2][4 * (i & 3) + 3], p, 3072); } } while (0)
    WO_BURST(0);
    TS(1, 3);
    ATT_RUN(3);
    WO_BURST(2);
    if (w >> 2) __builtin_amdgcn_s_setprio(0); else __builtin_amdgcn_s_setprio(1);
    ATT_RUN(3);
    asm volatile("s_barrier" ::: "memory");
    if (ABL2 != 1) {
    glds_run<5>(wp0 + (size_t)32 * 512, lds0 + K2_K + 5 * w * 1024);
    glds_run<5>(wp0 + (size_t)37 * 512, lds0 + K2_V + 5 * w * 1024);
    }
    WO_BURST(4);
    ATT_RUN(3);
    WO_BURST(6);
    {
        const u16* xp = x + (size_t)(rg * 8 + (c16 & 7)) * D + 64 * w + 4 * g4;
        GLD8(xr[0], xp, 0); GLD8(xr[1], xp, 32); GLD8(xr[2], xp, 64); GLD8(xr[3], xp, 96);
    }
    ATT_RUN(3);
    TS(1, 4);
    float l = sum_x32(O[8]);
    if (__any(!(l >= 5.9604645e-8f))) {
        asm volatile("s_waitcnt vmcnt(0)" ::: "memory");
        const u16* kg = KVf + lane * 8;
        const u16* vg = KVf + 64 * 512 + h * 128 + (r & 15) * 8;
        const unsigned cv = (r == 16) ? 0x3f803f80u : 0u;
        const u32x4 cst = {cv, cv, cv, cv};
        float mx = -INFINITY;
        const f32x16 z = {};
        for (int kb = 0; kb < 64; ++kb) {
            const f32x16 s = mfma32(*(const bf16x8*)(kg + kb * 512), qf, z);
#pragma unroll
            for (int i = 0; i < 16; ++i) mx = fmaxf(mx, s[i]);
        }
        mx = fmaxf(mx, __shfl_xor(mx, 32));
#pragma unroll
        for (int i = 0; i < 16; ++i) cinit[i] = -mx;
        O = f32x16{};
        for (int kb = 0; kb < 64; ++kb) {
            const f32x16 s = mfma32(*(const bf16x8*)(kg + kb * 512), qf, cinit);
            u32x4 v0 = *(const u32x4*)(vg + kb * 512), v1 = *(const u32x4*)(vg + kb * 512 + 256);
            if (!isv) { v0 = cst; v1 = cst; }
            u32x4 p0, p1;
#pragma unroll
            for (int i = 0; i < 4; ++i) { p0[i] = pack_bf16(EX(s[2 * i]), EX(s[2 * i + 1])); p1[i] = pack_bf16(EX(s[8 + 2 * i]), EX(s[8 + 2 * i + 1])); }
            O = mfma32(__builtin_bit_cast(bf16x8, v0), __builtin_bit_cast(bf16x8, p0), O);
            O = mfma32(__builtin_bit_cast(bf16x8, v1), __builtin_bit_cast(bf16x8, p1), O);
        }
        l = sum_x32(O[8]);
    }
    bf16x8 wl[22];
#pragma unroll
    for (int i = 0; i < 22; ++i) wl[i] = *(const bf16x8*)(wp0 + (size_t)(42 + i) * 512);
    {
        const float inv = __builtin_amdgcn_rcpf(l);
        lds_ptr dst = L + K2_Q + (r & 7) * K2_OSTR + ((4 * w + (r >> 3)) * 16 + 4 * h) * 2;
        u32x2 a, b;
        a[0] = pack_bf16(O[0] * inv, O[1] * inv); a[1] = pack_bf16(O[2] * inv, O[3] * inv);
        b[0] = pack_bf16(O[4] * inv, O[5] * inv); b[1] = pack_bf16(O[6] * inv, O[7] * inv);
        *(LDSP u32x2*)dst = a; *(LDSP u32x2*)(dst + 16) = b;
    }
    asm volatile("s_waitcnt vmcnt(22) lgkmcnt(0)\n\ts_barrier" ::: "memory");
    TS(1, 5);
#pragma unroll
    for (int i = 0; i < 16; ++i) { PIN(wf[0][i]); PIN(wf[1][i]); }
#pragma unroll
    for (int i = 0; i < 4; ++i) PIN(xr[i]);
    f32x4 acc[4] = {};
    lds_cptr ob = L + K2_Q + (c16 & 7) * K2_OSTR + g4 * 16;
#pragma unroll
    for (int s = 0; s < 16; ++s) {
        const bf16x8 b = ldsfrag(ob + s * 64);
        acc[0] = mfma16(wf[0][s], b, acc[0]);
        acc[1] = mfma16(wf[1][s], b, acc[1]);
        const bf16x8 a2 = s < 10 ? ldsfrag(L + (s < 5 ? K2_K + (5 * w + s) * 1024 : K2_V + (5 * w + s - 5) * 1024) + lane * 16) : wl[s - 10];
        acc[2] = mfma16(a2, b, acc[2]);
        acc[3] = mfma16(wl[6 + s], b, acc[3]);
    }
    PIN(acc[3]);
    TS(1, 6);
    float hf[16];
#pragma unroll
    for (int t = 0; t < 4; ++t) {
        hf[4 * t] = acc[t][0] + bf_lo(xr[t][0]); hf[4 * t + 1] = acc[t][1] + bf_hi(xr[t][0]);
        hf[4 * t + 2] = acc[t][2] + bf_lo(xr[t][1]); hf[4 * t + 3] = acc[t][3] + bf_hi(xr[t][1]);
    }
    float s1 = 0.f;
#pragma unroll
    for (int i = 0; i < 16; ++i) s1 += hf[i];
    s1 = sum_x32(sum_x16(s1));
    const float mw = s1 * (1.f / 64.f);
    float m2 = 0.f;
#pragma unroll
    for (int i = 0; i < 16; ++i) { const float d = hf[i] - mw; m2 += d * d; }
    m2 = sum_x32(sum_x16(m2));
    if (lane < 8) *(LDSP f32x2*)(L + K2_RED + (w * 8 + lane) * 8) = f32x2{mw, m2};
    const int row = rg * 8 + (c16 & 7);
    asm volatile("s_waitcnt lgkmcnt(0)\n\ts_barrier" ::: "memory");
    float mean = 0.f, M2 = 0.f;
    {
        f32x2 st[8];
#pragma unroll
        for (int ww = 0; ww < 8; ++ww) { st[ww] = *(LDSP const f32x2*)(L + K2_RED + (ww * 8 + (c16 & 7)) * 8); mean += st[ww][0]; }
        mean *= 0.125f;
#pragma unroll
        for (int ww = 0; ww < 8; ++ww) { const float d = st[ww][0] - mean; M2 += st[ww][1] + 64.f * d * d; }
    }
    const float rstd = __builtin_amdgcn_rsqf(M2 * (1.f / D) + LN_EPS);
    if (c16 < 8) {
#pragma unroll
        for (int t = 0; t < 4; ++t) {
            u32x2 o;
            o[0] = pack_bf16((hf[4 * t] - mean) * rstd, (hf[4 * t + 1] - mean) * rstd);
            o[1] = pack_bf16((hf[4 * t + 2] - mean) * rstd, (hf[4 * t + 3] - mean) * rstd);
            *(u32x2*)(HN + (((size_t)(row >> 5) * 32 + 4 * w + t) * 64 + (g4 >> 1) * 32 + (row & 31)) * 8 + 4 * (g4 & 1)) = o;
        }
#pragma unroll
        for (int t = 0; t < 4; ++t) *(f32x4*)(hbuf + (size_t)row * D + 64 * w + 16 * t + 4 * g4) = f32x4{hf[4 * t], hf[4 * t + 1], hf[4 * t + 2], hf[4 * t + 3]};
    }
    TS(1, 7);
}

constexpr int K4_LDS = 163840;
DI float gelu_tanh(float g) {
    constexpr float c0 = -2.f * 0.7978845608028654f * 1.4426950408889634f, c1 = c0 * 0.044715f;
    const float e = __builtin_amdgcn_exp2f(g * __builtin_fmaf(g * g, c1, c0));
    return g * __builtin_amdgcn_rcpf(1.f + e);
}
template <class Hook>
DI void k4_tile(int bid, const u16* __restrict__ HN, const u16* __restrict__ WguT, u16* __restrict__ HH, char* smem, Hook&& hook) {
    const unsigned lds0 = (unsigned)(uintptr_t)smem;
    lds_ptr L = (lds_ptr)smem;
    const int tid = threadIdx.x, lane = tid & 63, w = (int)rfl(tid >> 6);
    const int xcd = bid & 7, idx = bid >> 3;
    const int ib = (xcd >> 2) * 8 + (idx >> 2), jb = (xcd & 3) * 4 + (idx & 3);
    const int tb = 4 * ib + w;
    TS(2, 0);
    const u16* wsrc = WguT + (size_t)(7 * jb) * 32 * 512 + (size_t)w * 8 * 512 + lane * 8;
    auto issue_tile = [&](int t, int slot) {
        glds16x4(wsrc + (size_t)t * 32 * 512, lds0 + slot * 32768 + w * 8192);
        glds16x4(wsrc + ((size_t)t * 32 + 4) * 512, lds0 + slot * 32768 + w * 8192 + 4096);
    };
    bf16x8 xf[32];
    const u16* hp = HN + (size_t)tb * 32 * 512 + lane * 8;
    const u16* w0 = WguT + (size_t)(7 * jb) * 32 * 512 + (size_t)(2 * w) * 512 + lane * 8;
#define K4_QUARTER(q) do { \
        glds16(w0 + (size_t)(q) * 8 * 512, lds0 + (8 * (q) + 2 * w) * 1024); \
        glds16(w0 + (size_t)(q) * 8 * 512 + 512, lds0 + (8 * (q) + 2 * w + 1) * 1024); \
        _Pragma("unroll") for (int i = 2 * (q); i < 2 * (q) + 2; ++i) { const u16* p = hp + i * 2048; \
            GLD16(xf[4 * i], p, 0); GLD16(xf[4 * i + 1], p, 1024); GLD16(xf[4 * i + 2], p, 2048); GLD16(xf[4 * i + 3], p, 3072); } } while (0)
    K4_QUARTER(0); K4_QUARTER(1);
    f32x16 accp = {};
    bf16x8 fa[2][4];
    WAIT_VM_BAR(10);
#pragma unroll
    for (int i = 0; i < 8; ++i) PIN(xf[i]);
    TS(2, 1);
#pragma unroll
    for (int i = 0; i < 4; ++i) fa[0][i] = ldsfrag(L + lane * 16 + i * 1024);
#pragma unroll
    for (int t = 0; t < 8; ++t) {
        f32x16 acc = {};
        float v[8];
#pragma unroll
        for (int b = 0; b < 8; ++b) {
            if (t == 0) {
                if (b == 0) K4_QUARTER(2);
                if (b == 1) { WAIT_VM_BAR(10); K4_QUARTER(3); }
                if (b == 2) issue_tile(1, 1);
                if (b == 3) { WAIT_VM_BAR(18); issue_tile(2, 2); }
                if (b == 5) { WAIT_VM_BAR(16); issue_tile(3, 3); }
                if (b == 6) issue_tile(4, 4);
                if (b == 7) { WAIT_VM_BAR(24); }
                if ((b & 1) && b < 7) {
#pragma unroll
                    for (int i = 0; i < 8; ++i) PIN(xf[4 * (b + 1) + i]);
                }
            } else if (b == 3) {
                if (t == 1) { WAIT_VM_BAR(0); issue_tile(5, 0); }
                else if (t == 2) { asm volatile("s_waitcnt lgkmcnt(0)\n\ts_barrier" ::: "memory"); issue_tile(6, 1); }
                else if (t == 4) { WAIT_VM_BAR(34); }
            }
            if (t == 3 && b >= 3) hook(std::integral_constant<int, 0>{}, b - 3);
            if (t == 4 && b < 3) hook(std::integral_constant<int, 0>{}, b + 5);
            if (t < 7) {
                if (b < 7 || t < 6) {
                    lds_cptr nb = L + (b < 7 ? (t % 5) * 32768 + 4 * (b + 1) * 1024 : ((t + 1) % 5) * 32768) + lane * 16;
#pragma unroll
                    for (int i = 0; i < 4; ++i) fa[(b + 1) & 1][i] = ldsfrag(nb + i * 1024);
                }
                SBAR();
#pragma unroll
                for (int i = 0; i < 4; ++i) acc = mfma32(fa[b & 1][i], xf[4 * b + i], acc);
            }
            if (t > 0) { v[b] = gelu_tanh(accp[b]) * accp[b + 8]; PIN(v[b]); }
            SBAR();
        }
        if (t > 0) {
            u32x4 o;
            o[0] = pack_bf16(v[0], v[1]); o[1] = pack_bf16(v[2], v[3]); o[2] = pack_bf16(v[4], v[5]); o[3] = pack_bf16(v[6], v[7]);
            GST16_SC1(HH + ((size_t)tb * 112 + 7 * jb + t - 1) * 512 + lane * 8, o);
        }
        accp = acc;
        if (t < 6) TS(2, 2 + t);
    }
#undef K4_QUARTER
}

template <int I, int N, class F> DI void static_for(F&& f) {
    if constexpr (I < N) { f(std::integral_constant<int, I>{}); static_for<I + 1, N>(f); }
}
template <int N> DI void wait_vm() { asm volatile("s_waitcnt vmcnt(%0)" :: "i"(N) : "memory"); }
constexpr int k5_wait(int s, int PV, int PH) {
    int issued = 2 * PV + 2 * PH, pw = s < PV ? 2 * s + 1 : -1, ph = s < PH ? 2 * PV + 2 * s + 1 : -1;
    for (int t = 0; t < s; ++t) {
        if (t + PH < 28) { if (t + PH == s) ph = issued + 1; issued += 2; }
    }
    return issued - 1 - (pw > ph ? pw : ph);
}
#define GLD16_A(dst, ptr, imm) asm volatile("global_load_dwordx4 %0, %1, off offset:%2" : "=a"(dst) : "v"(ptr), "i"(imm))
#define PIN_A(x) asm volatile("" : "+a"(x))
#define GLD16_SC1(dst, ptr, imm) asm volatile("global_load_dwordx4 %0, %1, off offset:%2 sc1" : "=v"(dst) : "v"(ptr), "i"(imm))
__global__ __launch_bounds__(256, 1) void k45_ffn(const u16* __restrict__ HN, const u16* __restrict__ WguT, u16* __restrict__ HH,
                                                  const u16* __restrict__ WdT, const float* __restrict__ hbuf, u16* __restrict__ out,
                                                  unsigned* __restrict__ done) {
    extern __shared__ __attribute__((aligned(16))) char smem[];
    lds_ptr L = (lds_ptr)smem;
    const int tid = threadIdx.x, lane = tid & 63, w = (int)rfl(tid >> 6);
    const int bid = blockIdx.x;
    const int xcd = bid & 7, idx = bid >> 3, ib = xcd * 4 + (idx >> 3), j = idx & 7;
    const u16* a0 = WdT + ((size_t)(2 * j) * 112 + 28 * w) * 512 + lane * 8;
    const u16* a1 = a0 + (size_t)112 * 512;
    const u16* b0 = HH + ((size_t)(2 * ib) * 112 + 28 * w) * 512 + lane * 8;
    const u16* b1 = b0 + (size_t)112 * 512;
    constexpr int PW = 28, PV = 12, PH = 10;
    bf16x8 wa[PW][2];
    int job = bid;
    for (;;) {
        k4_tile(job, HN, WguT, HH, smem, [&](auto, int part) {
            static_for<0, 8>([&](auto pc) {
                constexpr int pp = decltype(pc)::value;
                if (part == pp) {
                    static_for<PV + 2 * pp, PV + 2 * pp + 2>([&](auto ic) {
                        constexpr int s = decltype(ic)::value;
                        const u16* pa = a0 + (s & ~3) * 512; const u16* pb = a1 + (s & ~3) * 512;
                        bf16x8& d0 = wa[s][0]; bf16x8& d1 = wa[s][1];
                        GLD16_A(d0, pa, (s & 3) * 1024); GLD16_A(d1, pb, (s & 3) * 1024);
                    });
                }
            });
        });
        TS(3, 0);
        asm volatile("s_waitcnt vmcnt(0) lgkmcnt(0)\n\ts_barrier" ::: "memory");
        if (tid == 0) {
            const int jx = job & 7, ji = job >> 3;
            const unsigned one = 1u;
            asm volatile("global_store_dword %0, %1, off sc1" :: "v"(done + ((jx >> 2) * 8 + (ji >> 2)) * 16 + (jx & 3) * 4 + (ji & 3)), "v"(one) : "memory");
        }
        static_for<0, PV>([&](auto ic) {
            constexpr int s = decltype(ic)::value;
            const u16* pa = a0 + (s & ~3) * 512; const u16* pb = a1 + (s & ~3) * 512;
            bf16x8& d0 = wa[s][0]; bf16x8& d1 = wa[s][1];
            GLD16(d0, pa, (s & 3) * 1024); GLD16(d1, pb, (s & 3) * 1024);
        });
        LDSP int* miss = (LDSP int*)L;
        if (w == 0) {
            int m = -1;
            const unsigned* fl = done + (ib >> 1) * 16 + (lane & 15);
            for (int spin = 0; spin < 4096; ++spin) {
                unsigned f;
                asm volatile("global_load_dword %0, %1, off sc1\n\ts_waitcnt vmcnt(0)" : "=v"(f) : "v"(fl) : "memory");
                const unsigned long long bad = __ballot(f == 0u) & 0xffffull;
                m = bad ? (int)__builtin_ctzll(bad) : -1;
                if (m < 0) break;
                __builtin_amdgcn_s_sleep(2);
            }
            if (lane == 0) *miss = m;
        }
        asm volatile("s_waitcnt lgkmcnt(0)\n\ts_barrier" ::: "memory");
        const int m = (int)rfl((unsigned)*miss);
        if (m < 0) break;
        asm volatile("s_waitcnt vmcnt(0) lgkmcnt(0)\n\ts_barrier" ::: "memory");
        const int ibp = ib >> 1, jbp = m;
        job = ((ibp & 7) * 4 + (jbp & 3)) * 8 + ((ibp >> 3) * 4 + (jbp >> 2));
    }
    TS(3, 1);
    const int r = lane & 31, h = lane >> 5;
    const int na = w >> 1, tbq = w & 1;
    const size_t base = (size_t)(ib * 64 + 32 * tbq + r) * D + 64 * j + 32 * na + 4 * h;
    f32x4 xv[4];
    { const float* hp = hbuf + base; GLD16(xv[0], hp, 0); GLD16(xv[1], hp, 32); GLD16(xv[2], hp, 64); GLD16(xv[3], hp, 96); }
    bf16x8 hb[PH][2];
#pragma unroll
    for (int s = 0; s < PH; ++s) { GLD16_SC1(hb[s][0], b0 + s * 512, 0); GLD16_SC1(hb[s][1], b1 + s * 512, 0); }
    f32x16 acc[2][2] = {};
    static_for<0, 28>([&](auto ic) {
        constexpr int s = decltype(ic)::value, sw = s % PW, sh = s % PH;
        wait_vm<k5_wait(s, PV, PH)>();
        bf16x8& w0 = wa[sw][0]; bf16x8& w1 = wa[sw][1]; bf16x8& h0 = hb[sh][0]; bf16x8& h1 = hb[sh][1];
        if constexpr (s < PV) { PIN(w0); PIN(w1); } else { PIN_A(w0); PIN_A(w1); }
        PIN(h0); PIN(h1);
        acc[0][0] = mfma32(wa[sw][0], hb[sh][0], acc[0][0]);
        acc[0][1] = mfma32(wa[sw][0], hb[sh][1], acc[0][1]);
        acc[1][0] = mfma32(wa[sw][1], hb[sh][0], acc[1][0]);
        acc[1][1] = mfma32(wa[sw][1], hb[sh][1], acc[1][1]);
        SBAR();
        const u16* pb0 = b0 + (s + PH) * 512; const u16* pb1 = b1 + (s + PH) * 512;
        if constexpr (s + PH < 28) { GLD16_SC1(h0, pb0, 0); GLD16_SC1(h1, pb1, 0); }
        SBAR();
    });
    TS(3, 2);
#pragma unroll
    for (int q = 0; q < 4; ++q) {
        if (q != w) {
            const int slot = (w - q + 3) & 3;
#pragma unroll
            for (int g = 0; g < 4; ++g) {
                f32x4 v = {acc[q >> 1][q & 1][4 * g], acc[q >> 1][q & 1][4 * g + 1], acc[q >> 1][q & 1][4 * g + 2], acc[q >> 1][q & 1][4 * g + 3]};
                *(LDSP f32x4*)(L + ((q * 3 + slot) * 4 + g) * 1024 + lane * 16) = v;
            }
        }
    }
    asm volatile("s_waitcnt lgkmcnt(0)\n\ts_barrier" ::: "memory");
#pragma unroll
    for (int g = 0; g < 4; ++g) PIN(xv[g]);
    f32x16 t = (w == 0) ? acc[0][0] : (w == 1) ? acc[0][1] : (w == 2) ? acc[1][0] : acc[1][1];
#pragma unroll
    for (int slot = 0; slot < 3; ++slot)
#pragma unroll
        for (int g = 0; g < 4; ++g) {
            const f32x4 v = *(LDSP const f32x4*)(L + ((w * 3 + slot) * 4 + g) * 1024 + lane * 16);
            t[4 * g] += v[0]; t[4 * g + 1] += v[1]; t[4 * g + 2] += v[2]; t[4 * g + 3] += v[3];
        }
#pragma unroll
    for (int g = 0; g < 4; ++g) {
        u32x2 ov;
        ov[0] = pack_bf16(t[4 * g] + xv[g][0], t[4 * g + 1] + xv[g][1]);
        ov[1] = pack_bf16(t[4 * g + 2] + xv[g][2], t[4 * g + 3] + xv[g][3]);
        *(u32x2*)(out + base + 8 * g) = ov;
    }
    TS(3, 3);
}

extern "C" void kernel_launch(void* const* d_in, const int* in_sizes, int n_in, void* d_out, int out_size, void* d_ws, size_t ws_size,
                              hipStream_t stream) {
    const u16* x = (const u16*)d_in[0];
    const u16* wq = (const u16*)d_in[1];
    const u16* wk = (const u16*)d_in[2];
    const u16* wv = (const u16*)d_in[3];
    const u16* wo = (const u16*)d_in[4];
    const u16* wg = (const u16*)d_in[6];
    const u16* wu = (const u16*)d_in[7];
    const u16* wd = (const u16*)d_in[8];
    char* ws = (char*)d_ws;
    u16* Qf = (u16*)(ws);
    u16* Kf = (u16*)(ws + (2 << 20));
    u16* Vf = (u16*)(ws + (2 << 20) + 65536);
    float* kmaxp = (float*)(ws + (2 << 20) + 131072);
    unsigned* done = (unsigned*)(ws + (2 << 20) + 196608);
    u16* WoT = (u16*)(ws + (3 << 20));
    u16* HN = (u16*)(ws + (4 << 20));
    float* hbuf = (float*)(ws + (24 << 20));
    u16* HH = (u16*)(ws + (8 << 20));
    u16* WguT = (u16*)(ws + (16 << 20));
    u16* WdT = (u16*)(ws + (20 << 20));
    (void)hipFuncSetAttribute((const void*)k1_ln_qkv, hipFuncAttributeMaxDynamicSharedMemorySize, K1_LDS);
    (void)hipFuncSetAttribute((const void*)k2_attn, hipFuncAttributeMaxDynamicSharedMemorySize, K2_LDS);
    (void)hipFuncSetAttribute((const void*)k45_ffn, hipFuncAttributeMaxDynamicSharedMemorySize, K4_LDS);
    k1_ln_qkv<<<256, 512, K1_LDS, stream>>>(x, wq, wk, wv, wo, wd, Qf, Kf, Vf, kmaxp, WoT, WdT, done);
    k2_attn<<<256, 512, K2_LDS, stream>>>(Qf, Kf, kmaxp, WoT, x, wg, wu, hbuf, HN, WguT);
    k45_ffn<<<256, 256, K4_LDS, stream>>>(HN, WguT, HH, WdT, hbuf, (u16*)d_out, done);
}
```

```cpp
#include <hip/hip_runtime.h>
#include <hip/hip_bf16.h>
#include <stdint.h>
#include <type_traits>

typedef unsigned short u16;
typedef __attribute__((ext_vector_type(8))) short bf16x8;
typedef __attribute__((ext_vector_type(4))) short s16x4;
typedef __attribute__((ext_vector_type(16))) float f32x16;
typedef __attribute__((ext_vector_type(4))) float f32x4;
typedef __attribute__((ext_vector_type(2))) float f32x2;
typedef __attribute__((ext_vector_type(4))) unsigned u32x4;
typedef __attribute__((ext_vector_type(2))) unsigned u32x2;
typedef __attribute__((ext_vector_type(2))) __bf16 bf16x2_t;

#define DI __device__ __forceinline__
#define LDSP __attribute__((address_space(3)))
typedef LDSP const char* lds_cptr;
typedef LDSP char* lds_ptr;

constexpr int S = 2048, D = 512, DF = 1792;
constexpr float LN_EPS = 1e-5f;
constexpr float QSCALE = 0.25f * 1.4426950408889634f;

DI unsigned pack_bf16(float a, float b) { f32x2 v = {a, b}; bf16x2_t r = __builtin_convertvector(v, bf16x2_t); return __builtin_bit_cast(unsigned, r); }
DI float bf_lo(unsigned u) { return __uint_as_float(u << 16); }
DI float bf_hi(unsigned u) { return __uint_as_float(u & 0xffff0000u); }
DI f32x16 mfma32(bf16x8 a, bf16x8 b, f32x16 c) { return __builtin_amdgcn_mfma_f32_32x32x16_bf16(a, b, c, 0, 0, 0); }
DI f32x4 mfma16(bf16x8 a, bf16x8 b, f32x4 c) { return __builtin_amdgcn_mfma_f32_16x16x32_bf16(a, b, c, 0, 0, 0); }
DI void glds16(const void* g, unsigned lds_base) {
    unsigned sv;
    asm volatile("s_mov_b32 %0, m0\n\ts_mov_b32 m0, %2\n\ts_nop 0\n\tglobal_load_lds_dwordx4 %1, off\n\ts_mov_b32 m0, %0"
                 : "=&s"(sv) : "v"(g), "s"(lds_base) : "memory");
}
DI void glds16x4(const void* g, unsigned lds_base) {
    unsigned sv;
    asm volatile("s_mov_b32 %0, m0\n\ts_mov_b32 m0, %2\n\ts_nop 0\n\tglobal_load_lds_dwordx4 %1, off\n\tglobal_load_lds_dwordx4 %1, off offset:1024\n\t"
                 "global_load_lds_dwordx4 %1, off offset:2048\n\tglobal_load_lds_dwordx4 %1, off offset:3072\n\ts_mov_b32 m0, %0"
                 : "=&s"(sv) : "v"(g), "s"(lds_base) : "memory");
}
template <int N> DI void glds_run(const u16* g, unsigned lds_base) {
#pragma unroll
    for (int i = 0; i + 4 <= N; i += 4) glds16x4(g + i * 512, lds_base + i * 1024);
#pragma unroll
    for (int i = N & ~3; i < N; ++i) glds16(g + i * 512, lds_base + i * 1024);
}
DI unsigned rfl(unsigned v) { return (unsigned)__builtin_amdgcn_readfirstlane((int)v); }
DI s16x4 trread(lds_cptr p) { return __builtin_amdgcn_ds_read_tr16_b64_v4i16((LDSP s16x4*)p); }
DI bf16x8 trfrag(lds_cptr p0, lds_cptr p1) { s16x4 a = trread(p0), b = trread(p1); return __builtin_shufflevector(a, b, 0, 1, 2, 3, 4, 5, 6, 7); }
DI bf16x8 ldsfrag(lds_cptr p) { return *(LDSP const bf16x8*)p; }
#define SBAR() __builtin_amdgcn_sched_barrier(0)
#define PIN(x) asm volatile("" : "+v"(x))
#define EX(x) __builtin_amdgcn_exp2f(x)
#define GLD16(dst, ptr, imm) asm volatile("global_load_dwordx4 %0, %1, off offset:%2" : "=v"(dst) : "v"(ptr), "i"(imm))
#define GST16_SC1(ptr, val) asm volatile("global_store_dwordx4 %0, %1, off sc1\n\ts_nop 1" :: "v"(ptr), "v"(val) : "memory")
#define GLD8(dst, ptr, imm) asm volatile("global_load_dwordx2 %0, %1, off offset:%2" : "=v"(dst) : "v"(ptr), "i"(imm))
#define WAIT_VM(N) asm volatile("s_waitcnt vmcnt(" #N ")" ::: "memory")
#define WAIT_VM_BAR(N) asm volatile("s_waitcnt vmcnt(" #N ") lgkmcnt(0)\n\ts_barrier" ::: "memory")

template <int CTRL> DI float dpp(float v) { return __int_as_float(__builtin_amdgcn_update_dpp(0, __float_as_int(v), CTRL, 0xf, 0xf, true)); }
DI float sum8(float v) { v += dpp<0xB1>(v); v += dpp<0x4E>(v); v += dpp<0x141>(v); return v; }
DI float max16(float v) { v = fmaxf(v, dpp<0xB1>(v)); v = fmaxf(v, dpp<0x4E>(v)); v = fmaxf(v, dpp<0x141>(v)); v = fmaxf(v, dpp<0x140>(v)); return v; }
DI float sum_x16(float v) { auto r = __builtin_amdgcn_permlane16_swap(__float_as_uint(v), __float_as_uint(v), false, false); return __uint_as_float(r[0]) + __uint_as_float(r[1]); }
DI float sum_x32(float v) { auto r = __builtin_amdgcn_permlane32_swap(__float_as_uint(v), __float_as_uint(v), false, false); return __uint_as_float(r[0]) + __uint_as_float(r[1]); }
DI float max_x16(float v) { auto r = __builtin_amdgcn_permlane16_swap(__float_as_uint(v), __float_as_uint(v), false, false); return fmaxf(__uint_as_float(r[0]), __uint_as_float(r[1])); }
DI float max_x32(float v) { auto r = __builtin_amdgcn_permlane32_swap(__float_as_uint(v), __float_as_uint(v), false, false); return fmaxf(__uint_as_float(r[0]), __uint_as_float(r[1])); }

DI void ln_rows8(const u32x4 (&v)[8], int lane, lds_ptr tile, int row0) {
    const int rr = lane >> 3, cc = lane & 7;
    f32x2 f[32];
#pragma unroll
    for (int c = 0; c < 8; ++c)
#pragma unroll
        for (int e = 0; e < 4; ++e) f[4 * c + e] = f32x2{bf_lo(v[c][e]), bf_hi(v[c][e])};
    f32x2 s2 = f[0];
#pragma unroll
    for (int i = 1; i < 32; ++i) s2 += f[i];
    float s = s2[0] + s2[1];
    s = sum8(s);
    const float mean = s * (1.f / D);
    const f32x2 m2 = {mean, mean};
    f32x2 q2 = {0.f, 0.f};
#pragma unroll
    for (int i = 0; i < 32; ++i) { f[i] -= m2; q2 += f[i] * f[i]; }
    float q = q2[0] + q2[1];
    q = sum8(q);
    const float rstd = __builtin_amdgcn_rsqf(q * (1.f / D) + LN_EPS);
    const f32x2 r2 = {rstd, rstd};
    const int row = row0 + rr;
#pragma unroll
    for (int c = 0; c < 8; ++c) {
        u32x4 o;
#pragma unroll
        for (int e = 0; e < 4; ++e) { const f32x2 t = f[4 * c + e] * r2; o[e] = pack_bf16(t[0], t[1]); }
        *(LDSP u32x4*)(tile + row * 1024 + (((cc + 8 * c) ^ (row & 15)) << 4)) = o;
    }
}


constexpr int K1_XN = 0, K1_WQ = 65536, K1_WKV = 131072, K1_ST = 147456, K1_LDS = K1_ST + 9216;
__global__ __launch_bounds__(512, 2) void k1_ln_qkv(const u16* __restrict__ x, const u16* __restrict__ wq, const u16* __restrict__ wk,
                                                  const u16* __restrict__ wv, const u16* __restrict__ wo, const u16* __restrict__ wd,
                                                  u16* __restrict__ Qf, u16* __restrict__ Kf, u16* __restrict__ Vf, float* __restrict__ kmaxp,
                                                  u16* __restrict__ WoT, u16* __restrict__ WdT, unsigned* __restrict__ done) {
    extern __shared__ __attribute__((aligned(16))) char smem[];
    const unsigned lds0 = (unsigned)(uintptr_t)smem;
    lds_ptr L = (lds_ptr)smem;
    const int tid = threadIdx.x, lane = tid & 63, w = (int)rfl(tid >> 6);
    const int bid = blockIdx.x, xcd = bid & 7, idx = bid >> 3, ib = xcd * 4 + (idx >> 3), j = idx & 7;
    const int row0 = ib * 64;
    if (tid == 0) done[bid] = 0u;
    u32x4 xv[8];
    {
        const u16* p = x + (size_t)(row0 + 8 * w + (lane >> 3)) * D + 8 * (lane & 7);
        GLD16(xv[0], p, 0); GLD16(xv[1], p, 128); GLD16(xv[2], p, 256); GLD16(xv[3], p, 384);
        GLD16(xv[4], p, 512); GLD16(xv[5], p, 640); GLD16(xv[6], p, 768); GLD16(xv[7], p, 896);
    }
    {
        const u16* src = wq + (size_t)((lane >> 2) & 7) * D + 64 * j + 32 * (lane >> 5) + 8 * (lane & 3);
#pragma unroll
        for (int n = 0; n < 8; ++n) { const int nn = w * 8 + n; glds16(src + (size_t)nn * 8 * D, lds0 + K1_WQ + nn * 1024); }
        const u16* s2 = ((j & 1) ? wv : wk) + lane * 8;
#pragma unroll
        for (int n = 0; n < 2; ++n) { const int nn = w * 2 + n; glds16(s2 + nn * 512, lds0 + K1_WKV + nn * 1024); }
        const int nt = bid & 15, sg = bid >> 4;
        {
            const u16* p = w < 7 ? wd + (size_t)(112 * sg + 16 * w + (lane >> 2)) * D + 32 * nt + 8 * (lane & 3)
                                 : wo + (size_t)(32 * sg + (lane >> 2)) * D + 32 * nt + 8 * (lane & 3);
            glds16(p, lds0 + K1_ST + w * 1024);
            if (w == 7) glds16(p + (size_t)16 * D, lds0 + K1_ST + 8 * 1024);
        }
    }
    WAIT_VM(11);
#pragma unroll
    for (int c = 0; c < 8; ++c) PIN(xv[c]);
    ln_rows8(xv, lane, L + K1_XN, 8 * w);
    WAIT_VM_BAR(0);
    const int r = lane & 31, h = lane >> 5, blk = (lane >> 4) & 1, q4 = (lane & 15) >> 2, p4 = lane & 3;
    const int wm = w & 1, wn = (w >> 1) & 1, kh = w >> 2;
    f32x16 acc = {};
    f32x4 akv = {};
    const int trow = 32 * wm + r;
    {
        lds_cptr wb = L + K1_WQ + kh * 32768 + h * 1024 + wn * 512 + q4 * 64 + 32 * blk + 8 * p4;
        lds_cptr xb = L + K1_XN + trow * 1024 + kh * 512;
        bf16x8 fa[16], fb[16];
#pragma unroll
        for (int i = 0; i < 16; ++i) { fa[i] = trfrag(wb + i * 2048, wb + i * 2048 + 256); fb[i] = ldsfrag(xb + ((((2 * i + h) & 15) ^ (trow & 15)) << 4) + (i >> 3) * 256); }
        const int g4 = lane >> 4, c16 = lane & 15, krow = 16 * (j >> 1) + c16;
        bf16x8 wf[2], xf[2];
#pragma unroll
        for (int ss = 0; ss < 2; ++ss) {
            const int s = 2 * w + ss;
            lds_cptr wp = L + K1_WKV + (32 * s + 8 * g4 + q4) * 32 + 8 * p4;
            wf[ss] = trfrag(wp, wp + 128);
            xf[ss] = ldsfrag(L + K1_XN + krow * 1024 + (((4 * s + g4) ^ (krow & 15)) << 4));
        }
#pragma unroll
        for (int i = 0; i < 16; ++i) acc = mfma32(fa[i], fb[i], acc);
#pragma unroll
        for (int ss = 0; ss < 2; ++ss) akv = (j & 1) ? mfma16(xf[ss], wf[ss], akv) : mfma16(wf[ss], xf[ss], akv);
    }
    {
        const int nt = bid & 15, sg = bid >> 4;
        if (w < 7) {
            lds_cptr p = L + K1_ST + w * 1024 + (4 * h + q4) * 64 + 32 * blk + 8 * p4;
            bf16x8 f = trfrag(p, p + 512);
            *(bf16x8*)(WdT + ((size_t)nt * 112 + 7 * sg + w) * 512 + lane * 8) = f;
        } else {
            const int g4 = lane >> 4;
#pragma unroll
            for (int n = 0; n < 2; ++n) {
                lds_cptr p = L + K1_ST + 7 * 1024 + (8 * g4 + q4) * 64 + 32 * n + 8 * p4;
                bf16x8 f = trfrag(p, p + 256);
                *(bf16x8*)(WoT + ((size_t)(2 * nt + n) * 16 + sg) * 512 + lane * 8) = f;
            }
        }
    }
    asm volatile("s_waitcnt lgkmcnt(0)\n\ts_barrier" ::: "memory");
    {
        LDSP f32x4* ex = (LDSP f32x4*)(L + K1_WQ + w * 2048 + lane * 16);
        const int o = kh ? 0 : 8;
        f32x4 e0, e1;
#pragma unroll
        for (int e = 0; e < 4; ++e) { e0[e] = kh ? acc[e] : acc[8 + e]; e1[e] = kh ? acc[4 + e] : acc[12 + e]; }
        (void)o;
        ex[0] = e0; ex[64] = e1;
        *(LDSP f32x4*)(L + K1_WQ + 16384 + w * 1024 + lane * 16) = akv;
    }
    __syncthreads();
    {
        LDSP const f32x4* ex = (LDSP const f32x4*)(L + K1_WQ + (w ^ 4) * 2048 + lane * 16);
        const f32x4 e0 = ex[0], e1 = ex[64];
        const int grow = row0 + trow;
#pragma unroll
        for (int gg = 0; gg < 2; ++gg) {
            const int g = 2 * kh + gg;
            float v[4];
#pragma unroll
            for (int e = 0; e < 4; ++e) v[e] = ((kh ? acc[8 + 4 * gg + e] : acc[4 * gg + e]) + (gg ? e1[e] : e0[e])) * QSCALE;
            const int head = (64 * j + 32 * wn) / 16 + (g >> 1);
            u32x2 o; o[0] = pack_bf16(v[0], v[1]); o[1] = pack_bf16(v[2], v[3]);
            u16* dst = Qf + ((((size_t)(grow >> 3) * 8 + (head >> 2)) * 2 + (g & 1)) * 32 + (head & 3) * 8 + (grow & 7)) * 8 + 4 * h;
            *(u32x2*)dst = o;
        }
    }
    if (w == 0) {
        const int g4 = lane >> 4, c16 = lane & 15;
        f32x4 t = akv;
#pragma unroll
        for (int ww = 1; ww < 8; ++ww) { f32x4 u = *(LDSP const f32x4*)(L + K1_WQ + 16384 + ww * 1024 + lane * 16); t += u; }
        u32x2 o; o[0] = pack_bf16(t[0], t[1]); o[1] = pack_bf16(t[2], t[3]);
        const int tok0 = row0 + 16 * (j >> 1);
        if (j & 1) {
            const int kb = tok0 >> 5, s = (tok0 >> 4) & 1;
            *(u32x2*)(Vf + ((((size_t)kb * 2 + s) * 2 + (g4 & 1)) * 16 + c16) * 8 + 4 * (g4 >> 1)) = o;
        } else {
            const int key = tok0 + c16;
            *(u32x2*)(Kf + (((size_t)(key >> 5) * 2 + (g4 >> 1)) * 32 + (key & 31)) * 8 + 4 * (g4 & 1)) = o;
            float a = bf_lo(o[0]), b = bf_hi(o[0]), c = bf_lo(o[1]), d = bf_hi(o[1]);
            float n2 = a * a + b * b + c * c + d * d;
            n2 = max16(sum_x32(sum_x16(n2)));
            if (lane == 0) kmaxp[tok0 >> 4] = n2;
        }
    }
}

constexpr int K2_K = 0, K2_V = 65536, K2_C = 131072, K2_Q = 135168, K2_KM = 143488, K2_ST = 144000, K2_RED = 158336, K2_LDS = 158848;
constexpr int K2_OSTR = 1040;
struct AttnState { f32x16 sA, sB, O; bf16x8 kf, va0, va1, vb0, vb1; lds_cptr kp, vp; };
#define EXP4(SC, o) do { _Pragma("unroll") for (int i = (o); i < (o) + 4; ++i) { SC[i] = EX(SC[i]); } PIN(SC); SBAR(); } while (0)
#define ATT_STEP(SC, SN, V0, V1, W0, W1, KOFF, VOFF) do { \
        u32x4 p0, p1; \
        EXP4(SC, 0); \
        SN = mfma32(kf, qf, cinit); PIN(SN); \
        kf = ldsfrag(kp + (KOFF)); W0 = ldsfrag(vp + (VOFF)); W1 = ldsfrag(vp + (VOFF) + 512); \
        SBAR(); \
        EXP4(SC, 4); \
        EXP4(SC, 8); \
        _Pragma("unroll") for (int i = 0; i < 4; ++i) p0[i] = pack_bf16(SC[2 * i], SC[2 * i + 1]); \
        PIN(p0); SBAR(); \
        O = mfma32(V0, __builtin_bit_cast(bf16x8, p0), O); PIN(O); \
        SBAR(); \
        EXP4(SC, 12); \
        _Pragma("unroll") for (int i = 0; i < 4; ++i) p1[i] = pack_bf16(SC[8 + 2 * i], SC[8 + 2 * i + 1]); \
        PIN(p1); SBAR(); \
        O = mfma32(V1, __builtin_bit_cast(bf16x8, p1), O); PIN(O); \
        SBAR(); \
    } while (0)
#define ATT_RUN(N4) for (int it = 0; it < (N4); ++it) { \
        ATT_STEP(sA, sB, va0, va1, vb0, vb1, 2048, 1024); \
        ATT_STEP(sB, sA, vb0, vb1, va0, va1, 3072, 2048); \
        ATT_STEP(sA, sB, va0, va1, vb0, vb1, 4096, 3072); \
        vp += vinc; \
        ATT_STEP(sB, sA, vb0, vb1, va0, va1, 5120, 0); \
        kp += 4096; }
#define ATT_INIT() do { kp = L + K2_K + lane * 16; vp = vp0; O = f32x16{}; \
        kf = ldsfrag(kp); va0 = ldsfrag(vp); va1 = ldsfrag(vp + 512); sA = mfma32(kf, qf, cinit); kf = ldsfrag(kp + 1024); SBAR(); } while (0)
__global__ __launch_bounds__(512, 2) void k2_attn(const u16* __restrict__ Qf, const u16* __restrict__ KVf,
                                               const float* __restrict__ kmaxp, const u16* __restrict__ WoT, const u16* __restrict__ x,
                                               const u16* __restrict__ wg, const u16* __restrict__ wu,
                                               float* __restrict__ hbuf, u16* __restrict__ HN, u16* __restrict__ WguT) {
    extern __shared__ __attribute__((aligned(16))) char smem[];
    const unsigned lds0 = (unsigned)(uintptr_t)smem;
    lds_ptr L = (lds_ptr)smem;
    const int tid = threadIdx.x, lane = tid & 63, w = (int)rfl(tid >> 6);
    const int rg = blockIdx.x;
    const int r = lane & 31, h = lane >> 5;
    glds16(Qf + (size_t)rg * 4096 + w * 512 + lane * 8, lds0 + K2_Q + w * 1024);
    if (lane < 32) glds16(kmaxp + lane * 4, lds0 + K2_KM);
    const int kv = (w >> 2) * 64, wq4 = w & 3;
    glds_run<5>(KVf + (size_t)(kv + 5 * wq4) * 512 + lane * 8, lds0 + K2_K + (kv + 5 * wq4) * 1024);
    const u16* kvrest = KVf + (size_t)(kv + 20 + 11 * wq4) * 512 + lane * 8;
    const unsigned kvdst = lds0 + K2_K + (kv + 20 + 11 * wq4) * 1024;
    const int sp = rg & 15, jb = rg >> 4;
    if (tid < 256) {
        const int b = tid >> 5, sl = tid & 31;
        const unsigned val = sl == 0 ? 0x3f803f80u : 0u;
        *(LDSP u32x4*)(L + K2_C + b * 512 + sl * 16) = u32x4{val, val, val, val};
    }
    WAIT_VM_BAR(0);
    glds_run<4>(kvrest, kvdst);
    const bf16x8 qf = ldsfrag(L + K2_Q + w * 1024 + lane * 16);
    float m;
    {
        float km = fmaxf(*(LDSP const float*)(L + K2_KM + lane * 4), *(LDSP const float*)(L + K2_KM + 256 + lane * 4));
        km = max_x32(max_x16(max16(km)));
        float q2 = 0.f;
        const u32x4 qu = __builtin_bit_cast(u32x4, qf);
#pragma unroll
        for (int e = 0; e < 4; ++e) { float a = bf_lo(qu[e]), b = bf_hi(qu[e]); q2 += a * a + b * b; }
        q2 = sum_x32(q2);
        m = __builtin_amdgcn_sqrtf(q2 * km) * 1.001f + 1e-6f;
    }
    f32x16 cinit;
#pragma unroll
    for (int i = 0; i < 16; ++i) cinit[i] = -m;
    const bool isv = r < 16;
    const int cslot = (r == 16) ? 0 : ((r >= 20 && r < 28) ? 64 : 16);
    const lds_cptr vp0 = isv ? (L + K2_V + h * 256 + r * 16) : (L + K2_C + cslot);
    const int vinc = isv ? 4096 : 0;
    f32x16 sA, sB, O; bf16x8 kf, va0, va1, vb0, vb1; lds_cptr kp, vp;
    if (w >> 2) __builtin_amdgcn_s_setprio(1);
    ATT_INIT();
    ATT_RUN(1);
    glds_run<4>(kvrest + 4 * 512, kvdst + 4096);
    ATT_RUN(1);
    glds_run<3>(kvrest + 8 * 512, kvdst + 8192);
    ATT_RUN(1);
    if (w < 7) {
#pragma unroll
        for (int n = 0; n < 2; ++n) {
            const int c = w * 64 + lane, row = c / 14, ch = c - row * 14;
            glds16((n ? wu : wg) + (size_t)(32 * sp + row) * DF + 112 * jb + 8 * ch, lds0 + K2_ST + n * 7168 + w * 1024);
        }
    }
    ATT_RUN(1);
    WAIT_VM_BAR(0);
    const int blk = (lane >> 4) & 1, q4 = (lane & 15) >> 2, p4 = lane & 3;
    if (w < 7) {
#pragma unroll
        for (int s2 = 0; s2 < 2; ++s2) {
            lds_cptr p = L + K2_ST + blk * 7168 + (16 * s2 + 8 * h + q4) * 224 + 32 * w + 8 * p4;
            bf16x8 f = trfrag(p, p + 4 * 224);
            *(bf16x8*)(WguT + ((size_t)(7 * jb + w) * 32 + 2 * sp + s2) * 512 + lane * 8) = f;
        }
    }
    const int c16 = lane & 15, g4 = lane >> 4;
    bf16x8 wf[2][16];
    u32x2 xr[4];
    const u16* wp0 = WoT + (size_t)(4 * w) * 16 * 512 + lane * 8;
#define WO_BURST(i0) do { _Pragma("unroll") for (int i = (i0); i < (i0) + 2; ++i) { const u16* p = wp0 + i * 2048; \
        GLD16(wf[i >> 2][4 * (i & 3)], p, 0); GLD16(wf[i >> 2][4 * (i & 3) + 1], p, 1024); GLD16(wf[i >> 2][4 * (i & 3) + 2], p, 2048); GLD16(wf[i >> 2][4 * (i & 3) + 3], p, 3072); } } while (0)
    WO_BURST(0);
    ATT_RUN(3);
    WO_BURST(2);
    if (w >> 2) __builtin_amdgcn_s_setprio(0); else __builtin_amdgcn_s_setprio(1);
    ATT_RUN(3);
    asm volatile("s_barrier" ::: "memory");
    glds_run<5>(wp0 + (size_t)32 * 512, lds0 + K2_K + 5 * w * 1024);
    glds_run<5>(wp0 + (size_t)37 * 512, lds0 + K2_V + 5 * w * 1024);
    WO_BURST(4);
    ATT_RUN(3);
    WO_BURST(6);
    {
        const u16* xp = x + (size_t)(rg * 8 + (c16 & 7)) * D + 64 * w + 4 * g4;
        GLD8(xr[0], xp, 0); GLD8(xr[1], xp, 32); GLD8(xr[2], xp, 64); GLD8(xr[3], xp, 96);
    }
    ATT_RUN(3);
    float l = sum_x32(O[8]);
    if (__any(!(l >= 5.9604645e-8f))) {
        asm volatile("s_waitcnt vmcnt(0)" ::: "memory");
        const u16* kg = KVf + lane * 8;
        const u16* vg = KVf + 64 * 512 + h * 128 + (r & 15) * 8;
        const unsigned cv = (r == 16) ? 0x3f803f80u : 0u;
        const u32x4 cst = {cv, cv, cv, cv};
        float mx = -INFINITY;
        const f32x16 z = {};
        for (int kb = 0; kb < 64; ++kb) {
            const f32x16 s = mfma32(*(const bf16x8*)(kg + kb * 512), qf, z);
#pragma unroll
            for (int i = 0; i < 16; ++i) mx = fmaxf(mx, s[i]);
        }
        mx = fmaxf(mx, __shfl_xor(mx, 32));
#pragma unroll
        for (int i = 0; i < 16; ++i) cinit[i] = -mx;
        O = f32x16{};
        for (int kb = 0; kb < 64; ++kb) {
            const f32x16 s = mfma32(*(const bf16x8*)(kg + kb * 512), qf, cinit);
            u32x4 v0 = *(const u32x4*)(vg + kb * 512), v1 = *(const u32x4*)(vg + kb * 512 + 256);
            if (!isv) { v0 = cst; v1 = cst; }
            u32x4 p0, p1;
#pragma unroll
            for (int i = 0; i < 4; ++i) { p0[i] = pack_bf16(EX(s[2 * i]), EX(s[2 * i + 1])); p1[i] = pack_bf16(EX(s[8 + 2 * i]), EX(s[8 + 2 * i + 1])); }
            O = mfma32(__builtin_bit_cast(bf16x8, v0), __builtin_bit_cast(bf16x8, p0), O);
            O = mfma32(__builtin_bit_cast(bf16x8, v1), __builtin_bit_cast(bf16x8, p1), O);
        }
        l = sum_x32(O[8]);
    }
    bf16x8 wl[22];
#pragma unroll
    for (int i = 0; i < 22; ++i) wl[i] = *(const bf16x8*)(wp0 + (size_t)(42 + i) * 512);
    {
        const float inv = __builtin_amdgcn_rcpf(l);
        lds_ptr dst = L + K2_Q + (r & 7) * K2_OSTR + ((4 * w + (r >> 3)) * 16 + 4 * h) * 2;
        u32x2 a, b;
        a[0] = pack_bf16(O[0] * inv, O[1] * inv); a[1] = pack_bf16(O[2] * inv, O[3] * inv);
        b[0] = pack_bf16(O[4] * inv, O[5] * inv); b[1] = pack_bf16(O[6] * inv, O[7] * inv);
        *(LDSP u32x2*)dst = a; *(LDSP u32x2*)(dst + 16) = b;
    }
    asm volatile("s_waitcnt vmcnt(22) lgkmcnt(0)\n\ts_barrier" ::: "memory");
#pragma unroll
    for (int i = 0; i < 16; ++i) { PIN(wf[0][i]); PIN(wf[1][i]); }
#pragma unroll
    for (int i = 0; i < 4; ++i) PIN(xr[i]);
    f32x4 acc[4] = {};
    lds_cptr ob = L + K2_Q + (c16 & 7) * K2_OSTR + g4 * 16;
#pragma unroll
    for (int s = 0; s < 16; ++s) {
        const bf16x8 b = ldsfrag(ob + s * 64);
        acc[0] = mfma16(wf[0][s], b, acc[0]);
        acc[1] = mfma16(wf[1][s], b, acc[1]);
        const bf16x8 a2 = s < 10 ? ldsfrag(L + (s < 5 ? K2_K + (5 * w + s) * 1024 : K2_V + (5 * w + s - 5) * 1024) + lane * 16) : wl[s - 10];
        acc[2] = mfma16(a2, b, acc[2]);
        acc[3] = mfma16(wl[6 + s], b, acc[3]);
    }
    PIN(acc[3]);
    float hf[16];
#pragma unroll
    for (int t = 0; t < 4; ++t) {
        hf[4 * t] = acc[t][0] + bf_lo(xr[t][0]); hf[4 * t + 1] = acc[t][1] + bf_hi(xr[t][0]);
        hf[4 * t + 2] = acc[t][2] + bf_lo(xr[t][1]); hf[4 * t + 3] = acc[t][3] + bf_hi(xr[t][1]);
    }
    float s1 = 0.f;
#pragma unroll
    for (int i = 0; i < 16; ++i) s1 += hf[i];
    s1 = sum_x32(sum_x16(s1));
    const float mw = s1 * (1.f / 64.f);
    float m2 = 0.f;
#pragma unroll
    for (int i = 0; i < 16; ++i) { const float d = hf[i] - mw; m2 += d * d; }
    m2 = sum_x32(sum_x16(m2));
    if (lane < 8) *(LDSP f32x2*)(L + K2_RED + (w * 8 + lane) * 8) = f32x2{mw, m2};
    const int row = rg * 8 + (c16 & 7);
    asm volatile("s_waitcnt lgkmcnt(0)\n\ts_barrier" ::: "memory");
    float mean = 0.f, M2 = 0.f;
    {
        f32x2 st[8];
#pragma unroll
        for (int ww = 0; ww < 8; ++ww) { st[ww] = *(LDSP const f32x2*)(L + K2_RED + (ww * 8 + (c16 & 7)) * 8); mean += st[ww][0]; }
        mean *= 0.125f;
#pragma unroll
        for (int ww = 0; ww < 8; ++ww) { const float d = st[ww][0] - mean; M2 += st[ww][1] + 64.f * d * d; }
    }
    const float rstd = __builtin_amdgcn_rsqf(M2 * (1.f / D) + LN_EPS);
    if (c16 < 8) {
#pragma unroll
        for (int t = 0; t < 4; ++t) {
            u32x2 o;
            o[0] = pack_bf16((hf[4 * t] - mean) * rstd, (hf[4 * t + 1] - mean) * rstd);
            o[1] = pack_bf16((hf[4 * t + 2] - mean) * rstd, (hf[4 * t + 3] - mean) * rstd);
            *(u32x2*)(HN + (((size_t)(row >> 5) * 32 + 4 * w + t) * 64 + (g4 >> 1) * 32 + (row & 31)) * 8 + 4 * (g4 & 1)) = o;
        }
#pragma unroll
        for (int t = 0; t < 4; ++t) *(f32x4*)(hbuf + (size_t)row * D + 64 * w + 16 * t + 4 * g4) = f32x4{hf[4 * t], hf[4 * t + 1], hf[4 * t + 2], hf[4 * t + 3]};
    }
}

constexpr int K4_LDS = 163840;
DI float gelu_tanh(float g) {
    constexpr float c0 = -2.f * 0.7978845608028654f * 1.4426950408889634f, c1 = c0 * 0.044715f;
    const float e = __builtin_amdgcn_exp2f(g * __builtin_fmaf(g * g, c1, c0));
    return g * __builtin_amdgcn_rcpf(1.f + e);
}
template <class Hook>
DI void k4_tile(int bid, const u16* __restrict__ HN, const u16* __restrict__ WguT, u16* __restrict__ HH, char* smem, Hook&& hook) {
    const unsigned lds0 = (unsigned)(uintptr_t)smem;
    lds_ptr L = (lds_ptr)smem;
    const int tid = threadIdx.x, lane = tid & 63, w = (int)rfl(tid >> 6);
    const int xcd = bid & 7, idx = bid >> 3;
    const int ib = (xcd >> 2) * 8 + (idx >> 2), jb = (xcd & 3) * 4 + (idx & 3);
    const int tb = 4 * ib + w;
    const u16* wsrc = WguT + (size_t)(7 * jb) * 32 * 512 + (size_t)w * 8 * 512 + lane * 8;
    auto issue_tile = [&](int t, int slot) {
        glds16x4(wsrc + (size_t)t * 32 * 512, lds0 + slot * 32768 + w * 8192);
        glds16x4(wsrc + ((size_t)t * 32 + 4) * 512, lds0 + slot * 32768 + w * 8192 + 4096);
    };
    bf16x8 xf[32];
    const u16* hp = HN + (size_t)tb * 32 * 512 + lane * 8;
    const u16* w0 = WguT + (size_t)(7 * jb) * 32 * 512 + (size_t)(2 * w) * 512 + lane * 8;
#define K4_QUARTER(q) do { \
        glds16(w0 + (size_t)(q) * 8 * 512, lds0 + (8 * (q) + 2 * w) * 1024); \
        glds16(w0 + (size_t)(q) * 8 * 512 + 512, lds0 + (8 * (q) + 2 * w + 1) * 1024); \
        _Pragma("unroll") for (int i = 2 * (q); i < 2 * (q) + 2; ++i) { const u16* p = hp + i * 2048; \
            GLD16(xf[4 * i], p, 0); GLD16(xf[4 * i + 1], p, 1024); GLD16(xf[4 * i + 2], p, 2048); GLD16(xf[4 * i + 3], p, 3072); } } while (0)
    K4_QUARTER(0); K4_QUARTER(1);
    f32x16 accp = {};
    bf16x8 fa[2][4];
    WAIT_VM_BAR(10);
#pragma unroll
    for (int i = 0; i < 8; ++i) PIN(xf[i]);
#pragma unroll
    for (int i = 0; i < 4; ++i) fa[0][i] = ldsfrag(L + lane * 16 + i * 1024);
#pragma unroll
    for (int t = 0; t < 8; ++t) {
        f32x16 acc = {};
        float v[8];
#pragma unroll
        for (int b = 0; b < 8; ++b) {
            if (t == 0) {
                if (b == 0) K4_QUARTER(2);
                if (b == 1) { WAIT_VM_BAR(10); K4_QUARTER(3); }
                if (b == 2) issue_tile(1, 1);
                if (b == 3) { WAIT_VM_BAR(18); issue_tile(2, 2); }
                if (b == 5) { WAIT_VM_BAR(16); issue_tile(3, 3); }
                if (b == 6) issue_tile(4, 4);
                if (b == 7) { WAIT_VM_BAR(24); }
                if ((b & 1) && b < 7) {
#pragma unroll
                    for (int i = 0; i < 8; ++i) PIN(xf[4 * (b + 1) + i]);
                }
            } else if (b == 3) {
                if (t == 1) { WAIT_VM_BAR(0); issue_tile(5, 0); }
                else if (t == 2) { asm volatile("s_waitcnt lgkmcnt(0)\n\ts_barrier" ::: "memory"); issue_tile(6, 1); }
                else if (t == 4) { WAIT_VM_BAR(34); }
            }
            if (t == 3 && b >= 3) hook(std::integral_constant<int, 0>{}, b - 3);
            if (t == 4 && b < 3) hook(std::integral_constant<int, 0>{}, b + 5);
            if (t < 7) {
                if (b < 7 || t < 6) {
                    lds_cptr nb = L + (b < 7 ? (t % 5) * 32768 + 4 * (b + 1) * 1024 : ((t + 1) % 5) * 32768) + lane * 16;
#pragma unroll
                    for (int i = 0; i < 4; ++i) fa[(b + 1) & 1][i] = ldsfrag(nb + i * 1024);
                }
                SBAR();
#pragma unroll
                for (int i = 0; i < 4; ++i) acc = mfma32(fa[b & 1][i], xf[4 * b + i], acc);
            }
            if (t > 0) { v[b] = gelu_tanh(accp[b]) * accp[b + 8]; PIN(v[b]); }
            SBAR();
        }
        if (t > 0) {
            u32x4 o;
            o[0] = pack_bf16(v[0], v[1]); o[1] = pack_bf16(v[2], v[3]); o[2] = pack_bf16(v[4], v[5]); o[3] = pack_bf16(v[6], v[7]);
            GST16_SC1(HH + ((size_t)tb * 112 + 7 * jb + t - 1) * 512 + lane * 8, o);
        }
        accp = acc;
    }
#undef K4_QUARTER
}

template <int I, int N, class F> DI void static_for(F&& f) {
    if constexpr (I < N) { f(std::integral_constant<int, I>{}); static_for<I + 1, N>(f); }
}
template <int N> DI void wait_vm() { asm volatile("s_waitcnt vmcnt(%0)" :: "i"(N) : "memory"); }
constexpr int k5_wait(int s, int PV, int PH) {
    int issued = 2 * PV + 2 * PH, pw = s < PV ? 2 * s + 1 : -1, ph = s < PH ? 2 * PV + 2 * s + 1 : -1;
    for (int t = 0; t < s; ++t) {
        if (t + PH < 28) { if (t + PH == s) ph = issued + 1; issued += 2; }
    }
    return issued - 1 - (pw > ph ? pw : ph);
}
#define GLD16_A(dst, ptr, imm) asm volatile("global_load_dwordx4 %0, %1, off offset:%2" : "=a"(dst) : "v"(ptr), "i"(imm))
#define PIN_A(x) asm volatile("" : "+a"(x))
#define GLD16_SC1(dst, ptr, imm) asm volatile("global_load_dwordx4 %0, %1, off offset:%2 sc1" : "=v"(dst) : "v"(ptr), "i"(imm))
__global__ __launch_bounds__(256, 1) void k45_ffn(const u16* __restrict__ HN, const u16* __restrict__ WguT, u16* __restrict__ HH,
                                                  const u16* __restrict__ WdT, const float* __restrict__ hbuf, u16* __restrict__ out,
                                                  unsigned* __restrict__ done) {
    extern __shared__ __attribute__((aligned(16))) char smem[];
    lds_ptr L = (lds_ptr)smem;
    const int tid = threadIdx.x, lane = tid & 63, w = (int)rfl(tid >> 6);
    const int bid = blockIdx.x;
    const int xcd = bid & 7, idx = bid >> 3, ib = xcd * 4 + (idx >> 3), j = idx & 7;
    const u16* a0 = WdT + ((size_t)(2 * j) * 112 + 28 * w) * 512 + lane * 8;
    const u16* a1 = a0 + (size_t)112 * 512;
    const u16* b0 = HH + ((size_t)(2 * ib) * 112 + 28 * w) * 512 + lane * 8;
    const u16* b1 = b0 + (size_t)112 * 512;
    constexpr int PW = 28, PV = 12, PH = 10;
    bf16x8 wa[PW][2];
    int job = bid;
    for (;;) {
        k4_tile(job, HN, WguT, HH, smem, [&](auto, int part) {
            static_for<0, 8>([&](auto pc) {
                constexpr int pp = decltype(pc)::value;
                if (part == pp) {
                    static_for<PV + 2 * pp, PV + 2 * pp + 2>([&](auto ic) {
                        constexpr int s = decltype(ic)::value;
                        const u16* pa = a0 + (s & ~3) * 512; const u16* pb = a1 + (s & ~3) * 512;
                        bf16x8& d0 = wa[s][0]; bf16x8& d1 = wa[s][1];
                        GLD16_A(d0, pa, (s & 3) * 1024); GLD16_A(d1, pb, (s & 3) * 1024);
                    });
                }
            });
        });
        asm volatile("s_waitcnt vmcnt(0) lgkmcnt(0)\n\ts_barrier" ::: "memory");
        if (tid == 0) {
            const int jx = job & 7, ji = job >> 3;
            const unsigned one = 1u;
            asm volatile("global_store_dword %0, %1, off sc1" :: "v"(done + ((jx >> 2) * 8 + (ji >> 2)) * 16 + (jx & 3) * 4 + (ji & 3)), "v"(one) : "memory");
        }
        static_for<0, PV>([&](auto ic) {
            constexpr int s = decltype(ic)::value;
            const u16* pa = a0 + (s & ~3) * 512; const u16* pb = a1 + (s & ~3) * 512;
            bf16x8& d0 = wa[s][0]; bf16x8& d1 = wa[s][1];
            GLD16(d0, pa, (s & 3) * 1024); GLD16(d1, pb, (s & 3) * 1024);
        });
        LDSP int* miss = (LDSP int*)L;
        if (w == 0) {
            int m = -1;
            const unsigned* fl = done + (ib >> 1) * 16 + (lane & 15);
            for (int spin = 0; spin < 4096; ++spin) {
                unsigned f;
                asm volatile("global_load_dword %0, %1, off sc1\n\ts_waitcnt vmcnt(0)" : "=v"(f) : "v"(fl) : "memory");
                const unsigned long long bad = __ballot(f == 0u) & 0xffffull;
                m = bad ? (int)__builtin_ctzll(bad) : -1;
                if (m < 0) break;
                __builtin_amdgcn_s_sleep(2);
            }
            if (lane == 0) *miss = m;
        }
        asm volatile("s_waitcnt lgkmcnt(0)\n\ts_barrier" ::: "memory");
        const int m = (int)rfl((unsigned)*miss);
        if (m < 0) break;
        asm volatile("s_waitcnt vmcnt(0) lgkmcnt(0)\n\ts_barrier" ::: "memory");
        const int ibp = ib >> 1, jbp = m;
        job = ((ibp & 7) * 4 + (jbp & 3)) * 8 + ((ibp >> 3) * 4 + (jbp >> 2));
    }
    const int r = lane & 31, h = lane >> 5;
    const int na = w >> 1, tbq = w & 1;
    const size_t base = (size_t)(ib * 64 + 32 * tbq + r) * D + 64 * j + 32 * na + 4 * h;
    f32x4 xv[4];
    { const float* hp = hbuf + base; GLD16(xv[0], hp, 0); GLD16(xv[1], hp, 32); GLD16(xv[2], hp, 64); GLD16(xv[3], hp, 96); }
    bf16x8 hb[PH][2];
#pragma unroll
    for (int s = 0; s < PH; ++s) { GLD16_SC1(hb[s][0], b0 + s * 512, 0); GLD16_SC1(hb[s][1], b1 + s * 512, 0); }
    f32x16 acc[2][2] = {};
    static_for<0, 28>([&](auto ic) {
        constexpr int s = decltype(ic)::value, sw = s % PW, sh = s % PH;
        wait_vm<k5_wait(s, PV, PH)>();
        bf16x8& w0 = wa[sw][0]; bf16x8& w1 = wa[sw][1]; bf16x8& h0 = hb[sh][0]; bf16x8& h1 = hb[sh][1];
        if constexpr (s < PV) { PIN(w0); PIN(w1); } else { PIN_A(w0); PIN_A(w1); }
        PIN(h0); PIN(h1);
        acc[0][0] = mfma32(wa[sw][0], hb[sh][0], acc[0][0]);
        acc[0][1] = mfma32(wa[sw][0], hb[sh][1], acc[0][1]);
        acc[1][0] = mfma32(wa[sw][1], hb[sh][0], acc[1][0]);
        acc[1][1] = mfma32(wa[sw][1], hb[sh][1], acc[1][1]);
        SBAR();
        const u16* pb0 = b0 + (s + PH) * 512; const u16* pb1 = b1 + (s + PH) * 512;
        if constexpr (s + PH < 28) { GLD16_SC1(h0, pb0, 0); GLD16_SC1(h1, pb1, 0); }
        SBAR();
    });
#pragma unroll
    for (int q = 0; q < 4; ++q) {
        if (q != w) {
            const int slot = (w - q + 3) & 3;
#pragma unroll
            for (int g = 0; g < 4; ++g) {
                f32x4 v = {acc[q >> 1][q & 1][4 * g], acc[q >> 1][q & 1][4 * g + 1], acc[q >> 1][q & 1][4 * g + 2], acc[q >> 1][q & 1][4 * g + 3]};
                *(LDSP f32x4*)(L + ((q * 3 + slot) * 4 + g) * 1024 + lane * 16) = v;
            }
        }
    }
    asm volatile("s_waitcnt lgkmcnt(0)\n\ts_barrier" ::: "memory");
#pragma unroll
    for (int g = 0; g < 4; ++g) PIN(xv[g]);
    f32x16 t = (w == 0) ? acc[0][0] : (w == 1) ? acc[0][1] : (w == 2) ? acc[1][0] : acc[1][1];
#pragma unroll
    for (int slot = 0; slot < 3; ++slot)
#pragma unroll
        for (int g = 0; g < 4; ++g) {
            const f32x4 v = *(LDSP const f32x4*)(L + ((w * 3 + slot) * 4 + g) * 1024 + lane * 16);
            t[4 * g] += v[0]; t[4 * g + 1] += v[1]; t[4 * g + 2] += v[2]; t[4 * g + 3] += v[3];
        }
#pragma unroll
    for (int g = 0; g < 4; ++g) {
        u32x2 ov;
        ov[0] = pack_bf16(t[4 * g] + xv[g][0], t[4 * g + 1] + xv[g][1]);
        ov[1] = pack_bf16(t[4 * g + 2] + xv[g][2], t[4 * g + 3] + xv[g][3]);
        *(u32x2*)(out + base + 8 * g) = ov;
    }
}

extern "C" void kernel_launch(void* const* d_in, const int* in_sizes, int n_in, void* d_out, int out_size, void* d_ws, size_t ws_size,
                              hipStream_t stream) {
    const u16* x = (const u16*)d_in[0];
    const u16* wq = (const u16*)d_in[1];
    const u16* wk = (const u16*)d_in[2];
    const u16* wv = (const u16*)d_in[3];
    const u16* wo = (const u16*)d_in[4];
    const u16* wg = (const u16*)d_in[6];
    const u16* wu = (const u16*)d_in[7];
    const u16* wd = (const u16*)d_in[8];
    char* ws = (char*)d_ws;
    u16* Qf = (u16*)(ws);
    u16* Kf = (u16*)(ws + (2 << 20));
    u16* Vf = (u16*)(ws + (2 << 20) + 65536);
    float* kmaxp = (float*)(ws + (2 << 20) + 131072);
    unsigned* done = (unsigned*)(ws + (2 << 20) + 196608);
    u16* WoT = (u16*)(ws + (3 << 20));
    u16* HN = (u16*)(ws + (4 << 20));
    float* hbuf = (float*)(ws + (24 << 20));
    u16* HH = (u16*)(ws + (8 << 20));
    u16* WguT = (u16*)(ws + (16 << 20));
    u16* WdT = (u16*)(ws + (20 << 20));
    (void)hipFuncSetAttribute((const void*)k1_ln_qkv, hipFuncAttributeMaxDynamicSharedMemorySize, K1_LDS);
    (void)hipFuncSetAttribute((const void*)k2_attn, hipFuncAttributeMaxDynamicSharedMemorySize, K2_LDS);
    (void)hipFuncSetAttribute((const void*)k45_ffn, hipFuncAttributeMaxDynamicSharedMemorySize, K4_LDS);
    k1_ln_qkv<<<256, 512, K1_LDS, stream>>>(x, wq, wk, wv, wo, wd, Qf, Kf, Vf, kmaxp, WoT, WdT, done);
    k2_attn<<<256, 512, K2_LDS, stream>>>(Qf, Kf, kmaxp, WoT, x, wg, wu, hbuf, HN, WguT);
    k45_ffn<<<256, 256, K4_LDS, stream>>>(HN, WguT, HH, WdT, hbuf, (u16*)d_out, done);
}
```

```cpp
#include <hip/hip_runtime.h>
#include <hip/hip_bf16.h>
#include <stdint.h>
#include <type_traits>

typedef unsigned short u16;
typedef __attribute__((ext_vector_type(8))) short bf16x8;
typedef __attribute__((ext_vector_type(4))) short s16x4;
typedef __attribute__((ext_vector_type(16))) float f32x16;
typedef __attribute__((ext_vector_type(4))) float f32x4;
typedef __attribute__((ext_vector_type(2))) float f32x2;
typedef __attribute__((ext_vector_type(4))) unsigned u32x4;
typedef __attribute__((ext_vector_type(2))) unsigned u32x2;
typedef __attribute__((ext_vector_type(2))) __bf16 bf16x2_t;

#define DI __device__ __forceinline__
#define LDSP __attribute__((address_space(3)))
typedef LDSP const char* lds_cptr;
typedef LDSP char* lds_ptr;

constexpr int S = 2048, D = 512, DF = 1792;
constexpr float LN_EPS = 1e-5f;
constexpr float QSCALE = 0.25f * 1.4426950408889634f;

DI unsigned pack_bf16(float a, float b) { f32x2 v = {a, b}; bf16x2_t r = __builtin_convertvector(v, bf16x2_t); return __builtin_bit_cast(unsigned, r); }
DI float bf_lo(unsigned u) { return __uint_as_float(u << 16); }
DI float bf_hi(unsigned u) { return __uint_as_float(u & 0xffff0000u); }
DI f32x16 mfma32(bf16x8 a, bf16x8 b, f32x16 c) { return __builtin_amdgcn_mfma_f32_32x32x16_bf16(a, b, c, 0, 0, 0); }
DI f32x4 mfma16(bf16x8 a, bf16x8 b, f32x4 c) { return __builtin_amdgcn_mfma_f32_16x16x32_bf16(a, b, c, 0, 0, 0); }
DI void glds16(const void* g, unsigned lds_base) {
    unsigned sv;
    asm volatile("s_mov_b32 %0, m0\n\ts_mov_b32 m0, %2\n\ts_nop 0\n\tglobal_load_lds_dwordx4 %1, off\n\ts_mov_b32 m0, %0"
                 : "=&s"(sv) : "v"(g), "s"(lds_base) : "memory");
}
DI void glds16x4(const void* g, unsigned lds_base) {
    unsigned sv;
    asm volatile("s_mov_b32 %0, m0\n\ts_mov_b32 m0, %2\n\ts_nop 0\n\tglobal_load_lds_dwordx4 %1, off\n\tglobal_load_lds_dwordx4 %1, off offset:1024\n\t"
                 "global_load_lds_dwordx4 %1, off offset:2048\n\tglobal_load_lds_dwordx4 %1, off offset:3072\n\ts_mov_b32 m0, %0"
                 : "=&s"(sv) : "v"(g), "s"(lds_base) : "memory");
}
template <int N> DI void glds_run(const u16* g, unsigned lds_base) {
#pragma unroll
    for (int i = 0; i + 4 <= N; i += 4) glds16x4(g + i * 512, lds_base + i * 1024);
#pragma unroll
    for (int i = N & ~3; i < N; ++i) glds16(g + i * 512, lds_base + i * 1024);
}
DI unsigned rfl(unsigned v) { return (unsigned)__builtin_amdgcn_readfirstlane((int)v); }
DI s16x4 trread(lds_cptr p) { return __builtin_amdgcn_ds_read_tr16_b64_v4i16((LDSP s16x4*)p); }
DI bf16x8 trfrag(lds_cptr p0, lds_cptr p1) { s16x4 a = trread(p0), b = trread(p1); return __builtin_shufflevector(a, b, 0, 1, 2, 3, 4, 5, 6, 7); }
DI bf16x8 ldsfrag(lds_cptr p) { return *(LDSP const bf16x8*)p; }
#define SBAR() __builtin_amdgcn_sched_barrier(0)
#define PIN(x) asm volatile("" : "+v"(x))
#define EX(x) __builtin_amdgcn_exp2f(x)
#define GLD16(dst, ptr, imm) asm volatile("global_load_dwordx4 %0, %1, off offset:%2" : "=v"(dst) : "v"(ptr), "i"(imm))
#define GST16_SC1(ptr, val) asm volatile("global_store_dwordx4 %0, %1, off sc1\n\ts_nop 1" :: "v"(ptr), "v"(val) : "memory")
#define GLD8(dst, ptr, imm) asm volatile("global_load_dwordx2 %0, %1, off offset:%2" : "=v"(dst) : "v"(ptr), "i"(imm))
#define WAIT_VM(N) asm volatile("s_waitcnt vmcnt(" #N ")" ::: "memory")
#define WAIT_VM_BAR(N) asm volatile("s_waitcnt vmcnt(" #N ") lgkmcnt(0)\n\ts_barrier" ::: "memory")

template <int CTRL> DI float dpp(float v) { return __int_as_float(__builtin_amdgcn_update_dpp(0, __float_as_int(v), CTRL, 0xf, 0xf, true)); }
DI float sum8(float v) { v += dpp<0xB1>(v); v += dpp<0x4E>(v); v += dpp<0x141>(v); return v; }
DI float max16(float v) { v = fmaxf(v, dpp<0xB1>(v)); v = fmaxf(v, dpp<0x4E>(v)); v = fmaxf(v, dpp<0x141>(v)); v = fmaxf(v, dpp<0x140>(v)); return v; }
DI float sum_x16(float v) { auto r = __builtin_amdgcn_permlane16_swap(__float_as_uint(v), __float_as_uint(v), false, false); return __uint_as_float(r[0]) + __uint_as_float(r[1]); }
DI float sum_x32(float v) { auto r = __builtin_amdgcn_permlane32_swap(__float_as_uint(v), __float_as_uint(v), false, false); return __uint_as_float(r[0]) + __uint_as_float(r[1]); }
DI float max_x16(float v) { auto r = __builtin_amdgcn_permlane16_swap(__float_as_uint(v), __float_as_uint(v), false, false); return fmaxf(__uint_as_float(r[0]), __uint_as_float(r[1])); }
DI float max_x32(float v) { auto r = __builtin_amdgcn_permlane32_swap(__float_as_uint(v), __float_as_uint(v), false, false); return fmaxf(__uint_as_float(r[0]), __uint_as_float(r[1])); }

DI void ln_rows8(const u32x4 (&v)[8], int lane, lds_ptr tile, int row0) {
    const int rr = lane >> 3, cc = lane & 7;
    f32x2 f[32];
#pragma unroll
    for (int c = 0; c < 8; ++c)
#pragma unroll
        for (int e = 0; e < 4; ++e) f[4 * c + e] = f32x2{bf_lo(v[c][e]), bf_hi(v[c][e])};
    f32x2 s2 = f[0];
#pragma unroll
    for (int i = 1; i < 32; ++i) s2 += f[i];
    float s = s2[0] + s2[1];
    s = sum8(s);
    const float mean = s * (1.f / D);
    const f32x2 m2 = {mean, mean};
    f32x2 q2 = {0.f, 0.f};
#pragma unroll
    for (int i = 0; i < 32; ++i) { f[i] -= m2; q2 += f[i] * f[i]; }
    float q = q2[0] + q2[1];
    q = sum8(q);
    const float rstd = __builtin_amdgcn_rsqf(q * (1.f / D) + LN_EPS);
    const f32x2 r2 = {rstd, rstd};
    const int row = row0 + rr;
#pragma unroll
    for (int c = 0; c < 8; ++c) {
        u32x4 o;
#pragma unroll
        for (int e = 0; e < 4; ++e) { const f32x2 t = f[4 * c + e] * r2; o[e] = pack_bf16(t[0], t[1]); }
        *(LDSP u32x4*)(tile + row * 1024 + (((cc + 8 * c) ^ (row & 15)) << 4)) = o;
    }
}


constexpr int K1_XN = 0, K1_WQ = 65536, K1_WKV = 131072, K1_ST = 147456, K1_LDS = K1_ST + 9216;
__global__ __launch_bounds__(512, 2) void k1_ln_qkv(const u16* __restrict__ x, const u16* __restrict__ wq, const u16* __restrict__ wk,
                                                  const u16* __restrict__ wv, const u16* __restrict__ wo, const u16* __restrict__ wd,
                                                  u16* __restrict__ Qf, u16* __restrict__ Kf, u16* __restrict__ Vf, float* __restrict__ kmaxp,
                                                  u16* __restrict__ WoT, u16* __restrict__ WdT, unsigned* __restrict__ done) {
    extern __shared__ __attribute__((aligned(16))) char smem[];
    const unsigned lds0 = (unsigned)(uintptr_t)smem;
    lds_ptr L = (lds_ptr)smem;
    const int tid = threadIdx.x, lane = tid & 63, w = (int)rfl(tid >> 6);
    const int bid = blockIdx.x, xcd = bid & 7, idx = bid >> 3, ib = xcd * 4 + (idx >> 3), j = idx & 7;
    const int row0 = ib * 64;
    if (tid == 0) done[bid] = 0u;
    u32x4 xv[8];
    {
        const u16* p = x + (size_t)(row0 + 8 * w + (lane >> 3)) * D + 8 * (lane & 7);
        GLD16(xv[0], p, 0); GLD16(xv[1], p, 128); GLD16(xv[2], p, 256); GLD16(xv[3], p, 384);
        GLD16(xv[4], p, 512); GLD16(xv[5], p, 640); GLD16(xv[6], p, 768); GLD16(xv[7], p, 896);
    }
    {
        const u16* src = wq + (size_t)((lane >> 2) & 7) * D + 64 * j + 32 * (lane >> 5) + 8 * (lane & 3);
#pragma unroll
        for (int n = 0; n < 8; ++n) { const int nn = w * 8 + n; glds16(src + (size_t)nn * 8 * D, lds0 + K1_WQ + nn * 1024); }
        const u16* s2 = ((j & 1) ? wv : wk) + lane * 8;
#pragma unroll
        for (int n = 0; n < 2; ++n) { const int nn = w * 2 + n; glds16(s2 + nn * 512, lds0 + K1_WKV + nn * 1024); }
        const int nt = bid & 15, sg = bid >> 4;
        {
            const u16* p = w < 7 ? wd + (size_t)(112 * sg + 16 * w + (lane >> 2)) * D + 32 * nt + 8 * (lane & 3)
                                 : wo + (size_t)(32 * sg + (lane >> 2)) * D + 32 * nt + 8 * (lane & 3);
            glds16(p, lds0 + K1_ST + w * 1024);
            if (w == 7) glds16(p + (size_t)16 * D, lds0 + K1_ST + 8 * 1024);
        }
    }
    WAIT_VM(11);
#pragma unroll
    for (int c = 0; c < 8; ++c) PIN(xv[c]);
    ln_rows8(xv, lane, L + K1_XN, 8 * w);
    WAIT_VM_BAR(0);
    const int r = lane & 31, h = lane >> 5, blk = (lane >> 4) & 1, q4 = (lane & 15) >> 2, p4 = lane & 3;
    const int wm = w & 1, wn = (w >> 1) & 1, kh = w >> 2;
    f32x16 acc = {};
    f32x4 akv = {};
    const int trow = 32 * wm + r;
    {
        lds_cptr wb = L + K1_WQ + kh * 32768 + h * 1024 + wn * 512 + q4 * 64 + 32 * blk + 8 * p4;
        lds_cptr xb = L + K1_XN + trow * 1024 + kh * 512;
        bf16x8 fa[16], fb[16];
#pragma unroll
        for (int i = 0; i < 16; ++i) { fa[i] = trfrag(wb + i * 2048, wb + i * 2048 + 256); fb[i] = ldsfrag(xb + ((((2 * i + h) & 15) ^ (trow & 15)) << 4) + (i >> 3) * 256); }
        const int g4 = lane >> 4, c16 = lane & 15, krow = 16 * (j >> 1) + c16;
        bf16x8 wf[2], xf[2];
#pragma unroll
        for (int ss = 0; ss < 2; ++ss) {
            const int s = 2 * w + ss;
            lds_cptr wp = L + K1_WKV + (32 * s + 8 * g4 + q4) * 32 + 8 * p4;
            wf[ss] = trfrag(wp, wp + 128);
            xf[ss] = ldsfrag(L + K1_XN + krow * 1024 + (((4 * s + g4) ^ (krow & 15)) << 4));
        }
#pragma unroll
        for (int i = 0; i < 16; ++i) acc = mfma32(fa[i], fb[i], acc);
#pragma unroll
        for (int ss = 0; ss < 2; ++ss) akv = (j & 1) ? mfma16(xf[ss], wf[ss], akv) : mfma16(wf[ss], xf[ss], akv);
    }
    {
        const int nt = bid & 15, sg = bid >> 4;
        if (w < 7) {
            lds_cptr p = L + K1_ST + w * 1024 + (4 * h + q4) * 64 + 32 * blk + 8 * p4;
            bf16x8 f = trfrag(p, p + 512);
            *(bf16x8*)(WdT + ((size_t)nt * 112 + 7 * sg + w) * 512 + lane * 8) = f;
        } else {
            const int g4 = lane >> 4;
#pragma unroll
            for (int n = 0; n < 2; ++n) {
                lds_cptr p = L + K1_ST + 7 * 1024 + (8 * g4 + q4) * 64 + 32 * n + 8 * p4;
                bf16x8 f = trfrag(p, p + 256);
                *(bf16x8*)(WoT + ((size_t)(2 * nt + n) * 16 + sg) * 512 + lane * 8) = f;
            }
        }
    }
    asm volatile("s_waitcnt lgkmcnt(0)\n\ts_barrier" ::: "memory");
    {
        LDSP f32x4* ex = (LDSP f32x4*)(L + K1_WQ + w * 2048 + lane * 16);
        const int o = kh ? 0 : 8;
        f32x4 e0, e1;
#pragma unroll
        for (int e = 0; e < 4; ++e) { e0[e] = kh ? acc[e] : acc[8 + e]; e1[e] = kh ? acc[4 + e] : acc[12 + e]; }
        (void)o;
        ex[0] = e0; ex[64] = e1;
        *(LDSP f32x4*)(L + K1_WQ + 16384 + w * 1024 + lane * 16) = akv;
    }
    __syncthreads();
    {
        LDSP const f32x4* ex = (LDSP const f32x4*)(L + K1_WQ + (w ^ 4) * 2048 + lane * 16);
        const f32x4 e0 = ex[0], e1 = ex[64];
        const int grow = row0 + trow;
#pragma unroll
        for (int gg = 0; gg < 2; ++gg) {
            const int g = 2 * kh + gg;
            float v[4];
#pragma unroll
            for (int e = 0; e < 4; ++e) v[e] = ((kh ? acc[8 + 4 * gg + e] : acc[4 * gg + e]) + (gg ? e1[e] : e0[e])) * QSCALE;
            const int head = (64 * j + 32 * wn) / 16 + (g >> 1);
            u32x2 o; o[0] = pack_bf16(v[0], v[1]); o[1] = pack_bf16(v[2], v[3]);
            u16* dst = Qf + ((((size_t)(grow >> 3) * 8 + (head >> 2)) * 2 + (g & 1)) * 32 + (head & 3) * 8 + (grow & 7)) * 8 + 4 * h;
            *(u32x2*)dst = o;
        }
    }
    if (w == 0) {
        const int g4 = lane >> 4, c16 = lane & 15;
        f32x4 t = akv;
#pragma unroll
        for (int ww = 1; ww < 8; ++ww) { f32x4 u = *(LDSP const f32x4*)(L + K1_WQ + 16384 + ww * 1024 + lane * 16); t += u; }
        u32x2 o; o[0] = pack_bf16(t[0], t[1]); o[1] = pack_bf16(t[2], t[3]);
        const int tok0 = row0 + 16 * (j >> 1);
        if (j & 1) {
            const int kb = tok0 >> 5, s = (tok0 >> 4) & 1;
            *(u32x2*)(Vf + ((((size_t)kb * 2 + s) * 2 + (g4 & 1)) * 16 + c16) * 8 + 4 * (g4 >> 1)) = o;
        } else {
            const int key = tok0 + c16;
            *(u32x2*)(Kf + (((size_t)(key >> 5) * 2 + (g4 >> 1)) * 32 + (key & 31)) * 8 + 4 * (g4 & 1)) = o;
            float a = bf_lo(o[0]), b = bf_hi(o[0]), c = bf_lo(o[1]), d = bf_hi(o[1]);
            float n2 = a * a + b * b + c * c + d * d;
            n2 = max16(sum_x32(sum_x16(n2)));
            if (lane == 0) kmaxp[tok0 >> 4] = n2;
        }
    }
}

constexpr int K2_K = 0, K2_V = 65536, K2_C = 131072, K2_Q = 135168, K2_KM = 143488, K2_ST = 144000, K2_RED = 158336, K2_LDS = 158848;
constexpr int K2_OSTR = 1040;
#define EXP4(SC, o) do { _Pragma("unroll") for (int i = (o); i < (o) + 4; ++i) { SC[i] = EX(SC[i]); } PIN(SC); SBAR(); } while (0)
#define ATT_STEP(SC, SN, V0, V1, W0, W1, KOFF, VOFF) do { \
        u32x4 p0, p1; \
        EXP4(SC, 0); \
        SN = mfma32(kf, qf, cinit); PIN(SN); \
        kf = ldsfrag(kp + (KOFF)); W0 = ldsfrag(vp + (VOFF)); W1 = ldsfrag(vp + (VOFF) + 512); \
        SBAR(); \
        EXP4(SC, 4); \
        EXP4(SC, 8); \
        _Pragma("unroll") for (int i = 0; i < 4; ++i) p0[i] = pack_bf16(SC[2 * i], SC[2 * i + 1]); \
        PIN(p0); SBAR(); \
        O = mfma32(V0, __builtin_bit_cast(bf16x8, p0), O); PIN(O); \
        SBAR(); \
        EXP4(SC, 12); \
        _Pragma("unroll") for (int i = 0; i < 4; ++i) p1[i] = pack_bf16(SC[8 + 2 * i], SC[8 + 2 * i + 1]); \
        PIN(p1); SBAR(); \
        O = mfma32(V1, __builtin_bit_cast(bf16x8, p1), O); PIN(O); \
        SBAR(); \
    } while (0)
#define ATT_RUN(N4) for (int it = 0; it < (N4); ++it) { \
        ATT_STEP(sA, sB, va0, va1, vb0, vb1, 2048, 1024); \
        ATT_STEP(sB, sA, vb0, vb1, va0, va1, 3072, 2048); \
        ATT_STEP(sA, sB, va0, va1, vb0, vb1, 4096, 3072); \
        vp += vinc; \
        ATT_STEP(sB, sA, vb0, vb1, va0, va1, 5120, 0); \
        kp += 4096; }
#define ATT_INIT() do { kp = L + K2_K + lane * 16; vp = vp0; O = f32x16{}; \
        kf = ldsfrag(kp); va0 = ldsfrag(vp); va1 = ldsfrag(vp + 512); sA = mfma32(kf, qf, cinit); kf = ldsfrag(kp + 1024); SBAR(); } while (0)
__global__ __launch_bounds__(512, 2) void k2_attn(const u16* __restrict__ Qf, const u16* __restrict__ KVf,
                                               const float* __restrict__ kmaxp, const u16* __restrict__ WoT, const u16* __restrict__ x,
                                               const u16* __restrict__ wg, const u16* __restrict__ wu,
                                               float* __restrict__ hbuf, u16* __restrict__ HN, u16* __restrict__ WguT) {
    extern __shared__ __attribute__((aligned(16))) char smem[];
    const unsigned lds0 = (unsigned)(uintptr_t)smem;
    lds_ptr L = (lds_ptr)smem;
    const int tid = threadIdx.x, lane = tid & 63, w = (int)rfl(tid >> 6);
    const int rg = blockIdx.x;
    const int r = lane & 31, h = lane >> 5;
    glds16(Qf + (size_t)rg * 4096 + w * 512 + lane * 8, lds0 + K2_Q + w * 1024);
    if (lane < 32) glds16(kmaxp + lane * 4, lds0 + K2_KM);
    const int kv = (w >> 2) * 64, wq4 = w & 3;
    glds_run<5>(KVf + (size_t)(kv + 5 * wq4) * 512 + lane * 8, lds0 + K2_K + (kv + 5 * wq4) * 1024);
    const u16* kvrest = KVf + (size_t)(kv + 20 + 11 * wq4) * 512 + lane * 8;
    const unsigned kvdst = lds0 + K2_K + (kv + 20 + 11 * wq4) * 1024;
    const int sp = rg & 15, jb = rg >> 4;
    if (tid < 256) {
        const int b = tid >> 5, sl = tid & 31;
        const unsigned val = sl == 0 ? 0x3f803f80u : 0u;
        *(LDSP u32x4*)(L + K2_C + b * 512 + sl * 16) = u32x4{val, val, val, val};
    }
    WAIT_VM_BAR(0);
    glds_run<4>(kvrest, kvdst);
    const bf16x8 qf = ldsfrag(L + K2_Q + w * 1024 + lane * 16);
    float m;
    {
        float km = fmaxf(*(LDSP const float*)(L + K2_KM + lane * 4), *(LDSP const float*)(L + K2_KM + 256 + lane * 4));
        km = max_x32(max_x16(max16(km)));
        float q2 = 0.f;
        const u32x4 qu = __builtin_bit_cast(u32x4, qf);
#pragma unroll
        for (int e = 0; e < 4; ++e) { float a = bf_lo(qu[e]), b = bf_hi(qu[e]); q2 += a * a + b * b; }
        q2 = sum_x32(q2);
        m = __builtin_amdgcn_sqrtf(q2 * km) * 1.001f + 1e-6f;
    }
    f32x16 cinit;
#pragma unroll
    for (int i = 0; i < 16; ++i) cinit[i] = -m;
    const bool isv = r < 16;
    const int cslot = (r == 16) ? 0 : ((r >= 20 && r < 28) ? 64 : 16);
    const lds_cptr vp0 = isv ? (L + K2_V + h * 256 + r * 16) : (L + K2_C + cslot);
    const int vinc = isv ? 4096 : 0;
    f32x16 sA, sB, O; bf16x8 kf, va0, va1, vb0, vb1; lds_cptr kp, vp;
    if (w >> 2) __builtin_amdgcn_s_setprio(1);
    ATT_INIT();
    ATT_RUN(1);
    glds_run<4>(kvrest + 4 * 512, kvdst + 4096);
    ATT_RUN(1);
    glds_run<3>(kvrest + 8 * 512, kvdst + 8192);
    ATT_RUN(1);
    if (w < 7) {
#pragma unroll
        for (int n = 0; n < 2; ++n) {
            const int c = w * 64 + lane, row = c / 14, ch = c - row * 14;
            glds16((n ? wu : wg) + (size_t)(32 * sp + row) * DF + 112 * jb + 8 * ch, lds0 + K2_ST + n * 7168 + w * 1024);
        }
    }
    ATT_RUN(1);
    WAIT_VM_BAR(0);
    const int blk = (lane >> 4) & 1, q4 = (lane & 15) >> 2, p4 = lane & 3;
    if (w < 7) {
#pragma unroll
        for (int s2 = 0; s2 < 2; ++s2) {
            lds_cptr p = L + K2_ST + blk * 7168 + (16 * s2 + 8 * h + q4) * 224 + 32 * w + 8 * p4;
            bf16x8 f = trfrag(p, p + 4 * 224);
            *(bf16x8*)(WguT + ((size_t)(7 * jb + w) * 32 + 2 * sp + s2) * 512 + lane * 8) = f;
        }
    }
    const int c16 = lane & 15, g4 = lane >> 4;
    bf16x8 wf[2][16];
    u32x2 xr[4];
    const u16* wp0 = WoT + (size_t)(4 * w) * 16 * 512 + lane * 8;
#define WO_BURST(i0) do { _Pragma("unroll") for (int i = (i0); i < (i0) + 2; ++i) { const u16* p = wp0 + i * 2048; \
        GLD16(wf[i >> 2][4 * (i & 3)], p, 0); GLD16(wf[i >> 2][4 * (i & 3) + 1], p, 1024); GLD16(wf[i >> 2][4 * (i & 3) + 2], p, 2048); GLD16(wf[i >> 2][4 * (i & 3) + 3], p, 3072); } } while (0)
    WO_BURST(0);
    ATT_RUN(3);
    WO_BURST(2);
    if (w >> 2) __builtin_amdgcn_s_setprio(0); else __builtin_amdgcn_s_setprio(1);
    ATT_RUN(3);
    asm volatile("s_barrier" ::: "memory");
    glds_run<5>(wp0 + (size_t)32 * 512, lds0 + K2_K + 5 * w * 1024);
    glds_run<5>(wp0 + (size_t)37 * 512, lds0 + K2_V + 5 * w * 1024);
    WO_BURST(4);
    ATT_RUN(3);
    WO_BURST(6);
    {
        const u16* xp = x + (size_t)(rg * 8 + (c16 & 7)) * D + 64 * w + 4 * g4;
        GLD8(xr[0], xp, 0); GLD8(xr[1], xp, 32); GLD8(xr[2], xp, 64); GLD8(xr[3], xp, 96);
    }
    ATT_RUN(3);
    float l = sum_x32(O[8]);
    if (__any(!(l >= 5.9604645e-8f))) {
        asm volatile("s_waitcnt vmcnt(0)" ::: "memory");
        const u16* kg = KVf + lane * 8;
        const u16* vg = KVf + 64 * 512 + h * 128 + (r & 15) * 8;
        const unsigned cv = (r == 16) ? 0x3f803f80u : 0u;
        const u32x4 cst = {cv, cv, cv, cv};
        float mx = -INFINITY;
        const f32x16 z = {};
        for (int kb = 0; kb < 64; ++kb) {
            const f32x16 s = mfma32(*(const bf16x8*)(kg + kb * 512), qf, z);
#pragma unroll
            for (int i = 0; i < 16; ++i) mx = fmaxf(mx, s[i]);
        }
        mx = fmaxf(mx, __shfl_xor(mx, 32));
#pragma unroll
        for (int i = 0; i < 16; ++i) cinit[i] = -mx;
        O = f32x16{};
        for (int kb = 0; kb < 64; ++kb) {
            const f32x16 s = mfma32(*(const bf16x8*)(kg + kb * 512), qf, cinit);
            u32x4 v0 = *(const u32x4*)(vg + kb * 512), v1 = *(const u32x4*)(vg + kb * 512 + 256);
            if (!isv) { v0 = cst; v1 = cst; }
            u32x4 p0, p1;
#pragma unroll
            for (int i = 0; i < 4; ++i) { p0[i] = pack_bf16(EX(s[2 * i]), EX(s[2 * i + 1])); p1[i] = pack_bf16(EX(s[8 + 2 * i]), EX(s[8 + 2 * i + 1])); }
            O = mfma32(__builtin_bit_cast(bf16x8, v0), __builtin_bit_cast(bf16x8, p0), O);
            O = mfma32(__builtin_bit_cast(bf16x8, v1), __builtin_bit_cast(bf16x8, p1), O);
        }
        l = sum_x32(O[8]);
    }
    bf16x8 wl[22];
#pragma unroll
    for (int i = 0; i < 22; ++i) wl[i] = *(const bf16x8*)(wp0 + (size_t)(42 + i) * 512);
    {
        const float inv = __builtin_amdgcn_rcpf(l);
        lds_ptr dst = L + K2_Q + (r & 7) * K2_OSTR + ((4 * w + (r >> 3)) * 16 + 4 * h) * 2;
        u32x2 a, b;
        a[0] = pack_bf16(O[0] * inv, O[1] * inv); a[1] = pack_bf16(O[2] * inv, O[3] * inv);
        b[0] = pack_bf16(O[4] * inv, O[5] * inv); b[1] = pack_bf16(O[6] * inv, O[7] * inv);
        *(LDSP u32x2*)dst = a; *(LDSP u32x2*)(dst + 16) = b;
    }
    asm volatile("s_waitcnt vmcnt(22) lgkmcnt(0)\n\ts_barrier" ::: "memory");
#pragma unroll
    for (int i = 0; i < 16; ++i) { PIN(wf[0][i]); PIN(wf[1][i]); }
#pragma unroll
    for (int i = 0; i < 4; ++i) PIN(xr[i]);
    f32x4 acc[4] = {};
    lds_cptr ob = L + K2_Q + (c16 & 7) * K2_OSTR + g4 * 16;
#pragma unroll
    for (int s = 0; s < 16; ++s) {
        const bf16x8 b = ldsfrag(ob + s * 64);
        acc[0] = mfma16(wf[0][s], b, acc[0]);
        acc[1] = mfma16(wf[1][s], b, acc[1]);
        const bf16x8 a2 = s < 10 ? ldsfrag(L + (s < 5 ? K2_K + (5 * w + s) * 1024 : K2_V + (5 * w + s - 5) * 1024) + lane * 16) : wl[s - 10];
        acc[2] = mfma16(a2, b, acc[2]);
        acc[3] = mfma16(wl[6 + s], b, acc[3]);
    }
    PIN(acc[3]);
    float hf[16];
#pragma unroll
    for (int t = 0; t < 4; ++t) {
        hf[4 * t] = acc[t][0] + bf_lo(xr[t][0]); hf[4 * t + 1] = acc[t][1] + bf_hi(xr[t][0]);
        hf[4 * t + 2] = acc[t][2] + bf_lo(xr[t][1]); hf[4 * t + 3] = acc[t][3] + bf_hi(xr[t][1]);
    }
    float s1 = 0.f;
#pragma unroll
    for (int i = 0; i < 16; ++i) s1 += hf[i];
    s1 = sum_x32(sum_x16(s1));
    const float mw = s1 * (1.f / 64.f);
    float m2 = 0.f;
#pragma unroll
    for (int i = 0; i < 16; ++i) { const float d = hf[i] - mw; m2 += d * d; }
    m2 = sum_x32(sum_x16(m2));
    if (lane < 8) *(LDSP f32x2*)(L + K2_RED + (w * 8 + lane) * 8) = f32x2{mw, m2};
    const int row = rg * 8 + (c16 & 7);
    asm volatile("s_waitcnt lgkmcnt(0)\n\ts_barrier" ::: "memory");
    float mean = 0.f, M2 = 0.f;
    {
        f32x2 st[8];
#pragma unroll
        for (int ww = 0; ww < 8; ++ww) { st[ww] = *(LDSP const f32x2*)(L + K2_RED + (ww * 8 + (c16 & 7)) * 8); mean += st[ww][0]; }
        mean *= 0.125f;
#pragma unroll
        for (int ww = 0; ww < 8; ++ww) { const float d = st[ww][0] - mean; M2 += st[ww][1] + 64.f * d * d; }
    }
    const float rstd = __builtin_amdgcn_rsqf(M2 * (1.f / D) + LN_EPS);
    if (c16 < 8) {
#pragma unroll
        for (int t = 0; t < 4; ++t) {
            u32x2 o;
            o[0] = pack_bf16((hf[4 * t] - mean) * rstd, (hf[4 * t + 1] - mean) * rstd);
            o[1] = pack_bf16((hf[4 * t + 2] - mean) * rstd, (hf[4 * t + 3] - mean) * rstd);
            *(u32x2*)(HN + (((size_t)(row >> 5) * 32 + 4 * w + t) * 64 + (g4 >> 1) * 32 + (row & 31)) * 8 + 4 * (g4 & 1)) = o;
        }
#pragma unroll
        for (int t = 0; t < 4; ++t) *(f32x4*)(hbuf + (size_t)row * D + 64 * w + 16 * t + 4 * g4) = f32x4{hf[4 * t], hf[4 * t + 1], hf[4 * t + 2], hf[4 * t + 3]};
    }
}

constexpr int K4_LDS = 163840;
DI float gelu_tanh(float g) {
    constexpr float c0 = -2.f * 0.7978845608028654f * 1.4426950408889634f, c1 = c0 * 0.044715f;
    const float e = __builtin_amdgcn_exp2f(g * __builtin_fmaf(g * g, c1, c0));
    return g * __builtin_amdgcn_rcpf(1.f + e);
}
template <class Hook>
DI void k4_tile(int bid, const u16* __restrict__ HN, const u16* __restrict__ WguT, u16* __restrict__ HH, char* smem, Hook&& hook) {
    const unsigned lds0 = (unsigned)(uintptr_t)smem;
    lds_ptr L = (lds_ptr)smem;
    const int tid = threadIdx.x, lane = tid & 63, w = (int)rfl(tid >> 6);
    const int xcd = bid & 7, idx = bid >> 3;
    const int ib = (xcd >> 2) * 8 + (idx >> 2), jb = (xcd & 3) * 4 + (idx & 3);
    const int tb = 4 * ib + w;
    const u16* wsrc = WguT + (size_t)(7 * jb) * 32 * 512 + (size_t)w * 8 * 512 + lane * 8;
    auto issue_tile = [&](int t, int slot) {
        glds16x4(wsrc + (size_t)t * 32 * 512, lds0 + slot * 32768 + w * 8192);
        glds16x4(wsrc + ((size_t)t * 32 + 4) * 512, lds0 + slot * 32768 + w * 8192 + 4096);
    };
    bf16x8 xf[32];
    const u16* hp = HN + (size_t)tb * 32 * 512 + lane * 8;
    const u16* w0 = WguT + (size_t)(7 * jb) * 32 * 512 + (size_t)(2 * w) * 512 + lane * 8;
#define K4_QUARTER(q) do { \
        glds16(w0 + (size_t)(q) * 8 * 512, lds0 + (8 * (q) + 2 * w) * 1024); \
        glds16(w0 + (size_t)(q) * 8 * 512 + 512, lds0 + (8 * (q) + 2 * w + 1) * 1024); \
        _Pragma("unroll") for (int i = 2 * (q); i < 2 * (q) + 2; ++i) { const u16* p = hp + i * 2048; \
            GLD16(xf[4 * i], p, 0); GLD16(xf[4 * i + 1], p, 1024); GLD16(xf[4 * i + 2], p, 2048); GLD16(xf[4 * i + 3], p, 3072); } } while (0)
    K4_QUARTER(0); K4_QUARTER(1);
    f32x16 accp = {};
    bf16x8 fa[2][4];
    WAIT_VM_BAR(10);
#pragma unroll
    for (int i = 0; i < 8; ++i) PIN(xf[i]);
#pragma unroll
    for (int i = 0; i < 4; ++i) fa[0][i] = ldsfrag(L + lane * 16 + i * 1024);
#pragma unroll
    for (int t = 0; t < 8; ++t) {
        f32x16 acc = {};
        float v[8];
#pragma unroll
        for (int b = 0; b < 8; ++b) {
            if (t == 0) {
                if (b == 0) K4_QUARTER(2);
                if (b == 1) { WAIT_VM_BAR(10); K4_QUARTER(3); }
                if (b == 2) issue_tile(1, 1);
                if (b == 3) { WAIT_VM_BAR(18); issue_tile(2, 2); }
                if (b == 5) { WAIT_VM_BAR(16); issue_tile(3, 3); }
                if (b == 6) issue_tile(4, 4);
                if (b == 7) { WAIT_VM_BAR(24); }
                if ((b & 1) && b < 7) {
#pragma unroll
                    for (int i = 0; i < 8; ++i) PIN(xf[4 * (b + 1) + i]);
                }
            } else if (b == 3) {
                if (t == 1) { WAIT_VM_BAR(0); issue_tile(5, 0); }
                else if (t == 2) { asm volatile("s_waitcnt lgkmcnt(0)\n\ts_barrier" ::: "memory"); issue_tile(6, 1); }
                else if (t == 4) { WAIT_VM_BAR(34); }
            }
            if (t == 3 && b >= 3) hook(std::integral_constant<int, 0>{}, b - 3);
            if (t == 4 && b < 3) hook(std::integral_constant<int, 0>{}, b + 5);
            if (t < 7) {
                if (b < 7 || t < 6) {
                    lds_cptr nb = L + (b < 7 ? (t % 5) * 32768 + 4 * (b + 1) * 1024 : ((t + 1) % 5) * 32768) + lane * 16;
#pragma unroll
                    for (int i = 0; i < 4; ++i) fa[(b + 1) & 1][i] = ldsfrag(nb + i * 1024);
                }
                SBAR();
#pragma unroll
                for (int i = 0; i < 4; ++i) acc = mfma32(fa[b & 1][i], xf[4 * b + i], acc);
            }
            if (t > 0) { v[b] = gelu_tanh(accp[b]) * accp[b + 8]; PIN(v[b]); }
            SBAR();
        }
        if (t > 0) {
            u32x4 o;
            o[0] = pack_bf16(v[0], v[1]); o[1] = pack_bf16(v[2], v[3]); o[2] = pack_bf16(v[4], v[5]); o[3] = pack_bf16(v[6], v[7]);
            GST16_SC1(HH + ((size_t)tb * 112 + 7 * jb + t - 1) * 512 + lane * 8, o);
        }
        accp = acc;
    }
#undef K4_QUARTER
}

template <int I, int N, class F> DI void static_for(F&& f) {
    if constexpr (I < N) { f(std::integral_constant<int, I>{}); static_for<I + 1, N>(f); }
}
template <int N> DI void wait_vm() { asm volatile("s_waitcnt vmcnt(%0)" :: "i"(N) : "memory"); }
constexpr int k5_wait(int s, int PV, int PH) {
    int issued = 2 * PV + 2 * PH, pw = s < PV ? 2 * s + 1 : -1, ph = s < PH ? 2 * PV + 2 * s + 1 : -1;
    for (int t = 0; t < s; ++t) {
        if (t + PH < 28) { if (t + PH == s) ph = issued + 1; issued += 2; }
    }
    return issued - 1 - (pw > ph ? pw : ph);
}
#define GLD16_A(dst, ptr, imm) asm volatile("global_load_dwordx4 %0, %1, off offset:%2" : "=a"(dst) : "v"(ptr), "i"(imm))
#define PIN_A(x) asm volatile("" : "+a"(x))
#define GLD16_SC1(dst, ptr, imm) asm volatile("global_load_dwordx4 %0, %1, off offset:%2 sc1" : "=v"(dst) : "v"(ptr), "i"(imm))
__global__ __launch_bounds__(256, 1) void k45_ffn(const u16* __restrict__ HN, const u16* __restrict__ WguT, u16* __restrict__ HH,
                                                  const u16* __restrict__ WdT, const float* __restrict__ hbuf, u16* __restrict__ out,
                                                  unsigned* __restrict__ done) {
    extern __shared__ __attribute__((aligned(16))) char smem[];
    lds_ptr L = (lds_ptr)smem;
    const int tid = threadIdx.x, lane = tid & 63, w = (int)rfl(tid >> 6);
    const int bid = blockIdx.x;
    const int xcd = bid & 7, idx = bid >> 3, ib = xcd * 4 + (idx >> 3), j = idx & 7;
    const u16* a0 = WdT + ((size_t)(2 * j) * 112 + 28 * w) * 512 + lane * 8;
    const u16* a1 = a0 + (size_t)112 * 512;
    const u16* b0 = HH + ((size_t)(2 * ib) * 112 + 28 * w) * 512 + lane * 8;
    const u16* b1 = b0 + (size_t)112 * 512;
    constexpr int PW = 28, PV = 12, PH = 10;
    bf16x8 wa[PW][2];
    int job = bid;
    for (;;) {
        k4_tile(job, HN, WguT, HH, smem, [&](auto, int part) {
            static_for<0, 8>([&](auto pc) {
                constexpr int pp = decltype(pc)::value;
                if (part == pp) {
                    static_for<PV + 2 * pp, PV + 2 * pp + 2>([&](auto ic) {
                        constexpr int s = decltype(ic)::value;
                        const u16* pa = a0 + (s & ~3) * 512; const u16* pb = a1 + (s & ~3) * 512;
                        bf16x8& d0 = wa[s][0]; bf16x8& d1 = wa[s][1];
                        GLD16_A(d0, pa, (s & 3) * 1024); GLD16_A(d1, pb, (s & 3) * 1024);
                    });
                }
            });
        });
        asm volatile("s_waitcnt vmcnt(0) lgkmcnt(0)\n\ts_barrier" ::: "memory");
        if (tid == 0) {
            const int jx = job & 7, ji = job >> 3;
            const unsigned one = 1u;
            asm volatile("global_store_dword %0, %1, off sc1" :: "v"(done + ((jx >> 2) * 8 + (ji >> 2)) * 16 + (jx & 3) * 4 + (ji & 3)), "v"(one) : "memory");
        }
        static_for<0, PV>([&](auto ic) {
            constexpr int s = decltype(ic)::value;
            const u16* pa = a0 + (s & ~3) * 512; const u16* pb = a1 + (s & ~3) * 512;
            bf16x8& d0 = wa[s][0]; bf16x8& d1 = wa[s][1];
            GLD16(d0, pa, (s & 3) * 1024); GLD16(d1, pb, (s & 3) * 1024);
        });
        LDSP int* miss = (LDSP int*)L;
        if (w == 0) {
            int m = -1;
            const unsigned* fl = done + (ib >> 1) * 16 + (lane & 15);
            for (int spin = 0; spin < 4096; ++spin) {
                unsigned f;
                asm volatile("global_load_dword %0, %1, off sc1\n\ts_waitcnt vmcnt(0)" : "=v"(f) : "v"(fl) : "memory");
                const unsigned long long bad = __ballot(f == 0u) & 0xffffull;
                m = bad ? (int)__builtin_ctzll(bad) : -1;
                if (m < 0) break;
                __builtin_amdgcn_s_sleep(2);
            }
            if (lane == 0) *miss = m;
        }
        asm volatile("s_waitcnt lgkmcnt(0)\n\ts_barrier" ::: "memory");
        const int m = (int)rfl((unsigned)*miss);
        if (m < 0) break;
        asm volatile("s_waitcnt vmcnt(0) lgkmcnt(0)\n\ts_barrier" ::: "memory");
        const int ibp = ib >> 1, jbp = m;
        job = ((ibp & 7) * 4 + (jbp & 3)) * 8 + ((ibp >> 3) * 4 + (jbp >> 2));
    }
    const int r = lane & 31, h = lane >> 5;
    const int na = w >> 1, tbq = w & 1;
    const size_t base = (size_t)(ib * 64 + 32 * tbq + r) * D + 64 * j + 32 * na + 4 * h;
    f32x4 xv[4];
    { const float* hp = hbuf + base; GLD16(xv[0], hp, 0); GLD16(xv[1], hp, 32); GLD16(xv[2], hp, 64); GLD16(xv[3], hp, 96); }
    bf16x8 hb[PH][2];
#pragma unroll
    for (int s = 0; s < PH; ++s) { GLD16_SC1(hb[s][0], b0 + s * 512, 0); GLD16_SC1(hb[s][1], b1 + s * 512, 0); }
    f32x16 acc[2][2] = {};
    static_for<0, 28>([&](auto ic) {
        constexpr int s = decltype(ic)::value, sw = s % PW, sh = s % PH;
        wait_vm<k5_wait(s, PV, PH)>();
        bf16x8& w0 = wa[sw][0]; bf16x8& w1 = wa[sw][1]; bf16x8& h0 = hb[sh][0]; bf16x8& h1 = hb[sh][1];
        if constexpr (s < PV) { PIN(w0); PIN(w1); } else { PIN_A(w0); PIN_A(w1); }
        PIN(h0); PIN(h1);
        acc[0][0] = mfma32(wa[sw][0], hb[sh][0], acc[0][0]);
        acc[0][1] = mfma32(wa[sw][0], hb[sh][1], acc[0][1]);
        acc[1][0] = mfma32(wa[sw][1], hb[sh][0], acc[1][0]);
        acc[1][1] = mfma32(wa[sw][1], hb[sh][1], acc[1][1]);
        SBAR();
        const u16* pb0 = b0 + (s + PH) * 512; const u16* pb1 = b1 + (s + PH) * 512;
        if constexpr (s + PH < 28) { GLD16_SC1(h0, pb0, 0); GLD16_SC1(h1, pb1, 0); }
        SBAR();
    });
#pragma unroll
    for (int q = 0; q < 4; ++q) {
        if (q != w) {
            const int slot = (w - q + 3) & 3;
#pragma unroll
            for (int g = 0; g < 4; ++g) {
                f32x4 v = {acc[q >> 1][q & 1][4 * g], acc[q >> 1][q & 1][4 * g + 1], acc[q >> 1][q & 1][4 * g + 2], acc[q >> 1][q & 1][4 * g + 3]};
                *(LDSP f32x4*)(L + ((q * 3 + slot) * 4 + g) * 1024 + lane * 16) = v;
            }
        }
    }
    asm volatile("s_waitcnt lgkmcnt(0)\n\ts_barrier" ::: "memory");
#pragma unroll
    for (int g = 0; g < 4; ++g) PIN(xv[g]);
    f32x16 t = (w == 0) ? acc[0][0] : (w == 1) ? acc[0][1] : (w == 2) ? acc[1][0] : acc[1][1];
#pragma unroll
    for (int slot = 0; slot < 3; ++slot)
#pragma unroll
        for (int g = 0; g < 4; ++g) {
            const f32x4 v = *(LDSP const f32x4*)(L + ((w * 3 + slot) * 4 + g) * 1024 + lane * 16);
            t[4 * g] += v[0]; t[4 * g + 1] += v[1]; t[4 * g + 2] += v[2]; t[4 * g + 3] += v[3];
        }
#pragma unroll
    for (int g = 0; g < 4; ++g) {
        u32x2 ov;
        ov[0] = pack_bf16(t[4 * g] + xv[g][0], t[4 * g + 1] + xv[g][1]);
        ov[1] = pack_bf16(t[4 * g + 2] + xv[g][2], t[4 * g + 3] + xv[g][3]);
        *(u32x2*)(out + base + 8 * g) = ov;
    }
}

extern "C" void kernel_launch(void* const* d_in, const int* in_sizes, int n_in, void* d_out, int out_size, void* d_ws, size_t ws_size,
                              hipStream_t stream) {
    const u16* x = (const u16*)d_in[0];
    const u16* wq = (const u16*)d_in[1];
    const u16* wk = (const u16*)d_in[2];
    const u16* wv = (const u16*)d_in[3];
    const u16* wo = (const u16*)d_in[4];
    const u16* wg = (const u16*)d_in[6];
    const u16* wu = (const u16*)d_in[7];
    const u16* wd = (const u16*)d_in[8];
    char* ws = (char*)d_ws;
    u16* Qf = (u16*)(ws);
    u16* Kf = (u16*)(ws + (size_t)2048 * 1024);
    u16* Vf = (u16*)(ws + (size_t)2112 * 1024);
    float* kmaxp = (float*)(ws + (size_t)2176 * 1024);
    unsigned* done = (unsigned*)(ws + (size_t)2178 * 1024);
    u16* WoT = (u16*)(ws + (size_t)2184 * 1024);
    u16* HN = (u16*)(ws + (size_t)2696 * 1024);
    u16* WdT = (u16*)(ws + (size_t)4744 * 1024);
    u16* WguT = (u16*)(ws + (size_t)6536 * 1024);
    float* hbuf = (float*)(ws + (size_t)10120 * 1024);
    u16* HH = (u16*)(ws + (size_t)14216 * 1024);
    (void)hipFuncSetAttribute((const void*)k1_ln_qkv, hipFuncAttributeMaxDynamicSharedMemorySize, K1_LDS);
    (void)hipFuncSetAttribute((const void*)k2_attn, hipFuncAttributeMaxDynamicSharedMemorySize, K2_LDS);
    (void)hipFuncSetAttribute((const void*)k45_ffn, hipFuncAttributeMaxDynamicSharedMemorySize, K4_LDS);
    k1_ln_qkv<<<256, 512, K1_LDS, stream>>>(x, wq, wk, wv, wo, wd, Qf, Kf, Vf, kmaxp, WoT, WdT, done);
    k2_attn<<<256, 512, K2_LDS, stream>>>(Qf, Kf, kmaxp, WoT, x, wg, wu, hbuf, HN, WguT);
    k45_ffn<<<256, 256, K4_LDS, stream>>>(HN, WguT, HH, WdT, hbuf, (u16*)d_out, done);
}
```
